```python
import torch

def _softmax(x, axis=-1):
    return torch.softmax(x, dim=axis)
def _gelu(x):
    return torch.nn.functional.gelu(x, approximate="tanh")
def _silu(x):
    return torch.nn.functional.silu(x)
def _sigmoid(x):
    return torch.sigmoid(x)
def _layernorm(x, axis=-1, eps=1e-5):
    m = x.mean(dim=axis, keepdim=True)
    v = x.var(dim=axis, keepdim=True, unbiased=False)
    return (x - m) / torch.sqrt(v + eps)
def _scatter_add(tgt, idx, src, axis=0):
    return tgt.clone().index_add_(axis, idx, src)
def _segment_sum(src, seg, n):
    out = torch.zeros((n,) + src.shape[1:], dtype=src.dtype)
    return out.index_add_(0, seg, src)
def _pad(x, pw):
    flat = tuple(v for pair in reversed(pw) for v in pair)
    return torch.nn.functional.pad(x, flat)

kernel_name = 'sequential_preLN__softmax_full_mha__dense_activation_relu__prompt'

def setup_inputs():

    _w = (lambda a: (lambda t: t.to(torch.bfloat16) if t.is_floating_point() else t)(torch.as_tensor(a)))
    def _A_setup():
        B, S, D = 1, 2048, 512
        return {
            "x": _w((lambda *s: torch.randn(*s, dtype=torch.bfloat16) * 0.05)(B, S, D)),
            "wqkv": _w((lambda *s: torch.randn(*s, dtype=torch.bfloat16) * 0.05)(D, 3 * D)),
            "wo": _w((lambda *s: torch.randn(*s, dtype=torch.bfloat16) * 0.05)(D, D)),
        }
    _A_in = _A_setup()

    def _B_setup():
        B, S, D, Dff = 1, 2048, 512, 1792
        return {
            "x": _w((lambda *s: torch.randn(*s, dtype=torch.bfloat16) * 0.05)(B, S, D)),
            "w1": _w((lambda *s: torch.randn(*s, dtype=torch.bfloat16) * 0.05)(D, Dff)),
            "w2": _w((lambda *s: torch.randn(*s, dtype=torch.bfloat16) * 0.05)(Dff, D)),
        }
    _B_in = _B_setup()

    return {
        "A_x": _A_in["x"],
        "A_wqkv": _A_in["wqkv"],
        "A_wo": _A_in["wo"],
        "B_x": _B_in["x"],
        "B_w1": _B_in["w1"],
        "B_w2": _B_in["w2"],
    }

def reference(A_x, A_wqkv, A_wo, B_x, B_w1, B_w2):
    def _A_ref(x, wqkv, wo):
        B, S, D = x.shape
        H = 32; Dh = D // H
        qkv = (lambda x, *d: x.permute(*d))((x @ wqkv).reshape(B, S, 3, H, Dh), 2, 0, 3, 1, 4)
        q, k, v = qkv[0], qkv[1], qkv[2]
        scale = Dh ** -0.5
        scores = torch.einsum("bhqd,bhkd->bhqk", q, k) * scale
        p = _softmax(scores, axis=-1)
        o = torch.einsum("bhqk,bhkd->bhqd", p, v)
        return (lambda x, *d: x.permute(*d))(o, 0, 2, 1, 3).reshape(B, S, D) @ wo

    def _B_ref(x, w1, w2):
        h = torch.einsum("bsd,df->bsf", x, w1)
        zero = torch.zeros_like(h)
        h = torch.maximum(h, zero)
        return torch.einsum("bsf,fd->bsd", h, w2)

    x = A_x
    h = x + _A_ref(_layernorm(x, axis=-1), A_wqkv, A_wo)
    h2 = h + _B_ref(_layernorm(h, axis=-1), B_w1, B_w2)
    return h2
```

```cpp
#include <hip/hip_runtime.h>
#include <hip/hip_bf16.h>
#include <stdint.h>

typedef unsigned short u16;
typedef __attribute__((ext_vector_type(8))) short s16x8;
typedef __attribute__((ext_vector_type(4))) short s16x4;
typedef __attribute__((ext_vector_type(16))) float f32x16;
typedef __attribute__((ext_vector_type(8))) float f32x8;
typedef __attribute__((ext_vector_type(4))) unsigned u32x4;
typedef __attribute__((ext_vector_type(8))) __bf16 bf16x8;
typedef __attribute__((ext_vector_type(16))) __bf16 bf16x16;

#define S_LEN 2048
#define DM 512
#define NH 32
#define DH 16
#define DFF 1792

__device__ __forceinline__ float bf2f(u16 v) { return __uint_as_float(((unsigned)v) << 16); }
__device__ __forceinline__ u16 f2bf(float f) { __bf16 b = (__bf16)f; return __builtin_bit_cast(u16, b); }

__global__ __launch_bounds__(256) void ln_kernel(const u16* __restrict__ x, u16* __restrict__ y) {
  int row = blockIdx.x * 4 + (threadIdx.x >> 6), lane = threadIdx.x & 63;
  s16x8 v = *(const s16x8*)(x + row * DM + lane * 8);
  float f[8], s = 0;
  for (int i = 0; i < 8; i++) { f[i] = bf2f((u16)v[i]); s += f[i]; }
  for (int m = 32; m; m >>= 1) s += __shfl_xor(s, m);
  float mean = s * (1.f / DM), q = 0;
  for (int i = 0; i < 8; i++) { f[i] -= mean; q += f[i] * f[i]; }
  for (int m = 32; m; m >>= 1) q += __shfl_xor(q, m);
  float r = rsqrtf(q * (1.f / DM) + 1e-5f);
  s16x8 o; for (int i = 0; i < 8; i++) o[i] = (short)f2bf(f[i] * r);
  *(s16x8*)(y + row * DM + lane * 8) = o;
}

template <int EPI>
__global__ __launch_bounds__(256) void gemm_kernel(const u16* __restrict__ A, const u16* __restrict__ W, int K, int N,
                                                   const u16* __restrict__ res, u16* __restrict__ out, u16* __restrict__ out2, u16* __restrict__ out3) {
  __shared__ __attribute__((aligned(16))) u16 sA[64][72];
  __shared__ __attribute__((aligned(16))) u16 sW[64][72];
  int tn = blockIdx.x, tm = blockIdx.y;
  int tid = threadIdx.x, lane = tid & 63, w = tid >> 6, wn = w & 1, wm = w >> 1;
  int r = lane & 31, h = lane >> 5;
  f32x16 acc; for (int i = 0; i < 16; i++) acc[i] = 0;
  for (int k0 = 0; k0 < K; k0 += 64) {
    for (int i = 0; i < 2; i++) {
      int c = tid + i * 256, row = c >> 3, ch = c & 7;
      *(s16x8*)&sA[row][ch * 8] = *(const s16x8*)(A + (size_t)(tm * 64 + row) * K + k0 + ch * 8);
      *(s16x8*)&sW[row][ch * 8] = *(const s16x8*)(W + (size_t)(k0 + row) * N + tn * 64 + ch * 8);
    }
    __syncthreads();
    for (int ks = 0; ks < 4; ks++) {
      s16x8 b = *(const s16x8*)&sA[wm * 32 + r][ks * 16 + h * 8];
      s16x8 a; for (int j = 0; j < 8; j++) a[j] = (short)sW[ks * 16 + h * 8 + j][wn * 32 + r];
      acc = __builtin_amdgcn_mfma_f32_32x32x16_bf16(__builtin_bit_cast(bf16x8, a), __builtin_bit_cast(bf16x8, b), acc, 0, 0, 0);
    }
    __syncthreads();
  }
  int m = tm * 64 + wm * 32 + r;
  for (int i = 0; i < 16; i++) {
    int n = tn * 64 + wn * 32 + (i & 3) + 8 * (i >> 2) + 4 * h;
    float v = acc[i];
    if (EPI == 0) {
      int which = n / DM, c = n % DM, hd = c / DH, d = c % DH;
      if (which == 0) out[((size_t)hd * S_LEN + m) * DH + d] = f2bf(v * (0.25f * 1.4426950408889634f));
      else if (which == 1) out2[((size_t)hd * S_LEN + m) * DH + d] = f2bf(v);
      else out3[((size_t)hd * DH + d) * S_LEN + m] = f2bf(v);
    } else if (EPI == 1) {
      out[(size_t)m * N + n] = f2bf(bf2f(f2bf(v)) + bf2f(res[(size_t)m * N + n]));
    } else {
      out[(size_t)m * N + n] = f2bf(fmaxf(v, 0.f));
    }
  }
}

__global__ __launch_bounds__(512) void attn_kernel(const u16* __restrict__ Q, const u16* __restrict__ Kx, const u16* __restrict__ Vt, u16* __restrict__ O) {
  int hd = blockIdx.x >> 3, qb = blockIdx.x & 7;
  int tid = threadIdx.x, lane = tid & 63, w = tid >> 6, r = lane & 31, h = lane >> 5;
  int q = qb * 256 + w * 32 + r;
  bf16x8 qf = *(const bf16x8*)(Q + ((size_t)hd * S_LEN + q) * DH + h * 8);
  f32x16 o; for (int i = 0; i < 16; i++) o[i] = 0;
  float m = -1e30f;
  bf16x8 ones; for (int i = 0; i < 8; i++) ones[i] = (__bf16)1.0f;
  for (int k0 = 0; k0 < S_LEN; k0 += 32) {
    bf16x8 kf = *(const bf16x8*)(Kx + ((size_t)hd * S_LEN + k0 + r) * DH + h * 8);
    f32x16 z; for (int i = 0; i < 16; i++) z[i] = 0;
    f32x16 s = __builtin_amdgcn_mfma_f32_32x32x16_bf16(kf, qf, z, 0, 0, 0);
    float mx = s[0]; for (int i = 1; i < 16; i++) mx = fmaxf(mx, s[i]);
    mx = fmaxf(mx, __shfl_xor(mx, 32));
    float mn = fmaxf(m, mx), al = __builtin_amdgcn_exp2f(m - mn); m = mn;
    for (int i = 0; i < 16; i++) { o[i] *= al; s[i] = __builtin_amdgcn_exp2f(s[i] - mn); }
    for (int t = 0; t < 2; t++) {
      bf16x8 vf = ones;
      if (r < 16) vf = *(const bf16x8*)(Vt + ((size_t)hd * DH + r) * S_LEN + k0 + t * 16 + h * 8);
      f32x8 p; for (int i = 0; i < 8; i++) p[i] = s[t * 8 + i];
      o = __builtin_amdgcn_smfmac_f32_32x32x32_bf16(vf, __builtin_bit_cast(bf16x16, p), o, 0xDDDD, 0, 0);
    }
  }
  float inv = 1.f / o[8];
  for (int i = 0; i < 8; i++) { int d = (i & 3) + 8 * (i >> 2) + 4 * h; O[(size_t)q * DM + hd * DH + d] = f2bf(o[i] * inv); }
}

extern "C" void kernel_launch(void* const* d_in, const int* in_sizes, int n_in, void* d_out, int out_size, void* d_ws, size_t ws_size, hipStream_t stream) {
  const u16* x = (const u16*)d_in[0]; const u16* wqkv = (const u16*)d_in[1]; const u16* wo = (const u16*)d_in[2];
  const u16* w1 = (const u16*)d_in[4]; const u16* w2 = (const u16*)d_in[5];
  u16* ws = (u16*)d_ws; const size_t T = (size_t)S_LEN * DM;
  u16 *xn = ws, *Q = ws + T, *Kx = ws + 2 * T, *Vt = ws + 3 * T, *O = ws + 4 * T, *hb = ws + 5 * T, *hn = ws + 6 * T, *u = ws + 7 * T;
  ln_kernel<<<S_LEN / 4, 256, 0, stream>>>(x, xn);
  gemm_kernel<0><<<dim3(3 * DM / 64, S_LEN / 64), 256, 0, stream>>>(xn, wqkv, DM, 3 * DM, nullptr, Q, Kx, Vt);
  attn_kernel<<<256, 512, 0, stream>>>(Q, Kx, Vt, O);
  gemm_kernel<1><<<dim3(DM / 64, S_LEN / 64), 256, 0, stream>>>(O, wo, DM, DM, x, hb, nullptr, nullptr);
  ln_kernel<<<S_LEN / 4, 256, 0, stream>>>(hb, hn);
  gemm_kernel<2><<<dim3(DFF / 64, S_LEN / 64), 256, 0, stream>>>(hn, w1, DM, DFF, nullptr, u, nullptr, nullptr);
  gemm_kernel<1><<<dim3(DM / 64, S_LEN / 64), 256, 0, stream>>>(u, w2, DFF, DM, hb, (u16*)d_out, nullptr, nullptr);
}
```

```cpp
#include <hip/hip_runtime.h>
#include <stdint.h>

typedef unsigned short u16;
typedef __attribute__((ext_vector_type(2))) __bf16 bf16x2;
typedef __attribute__((ext_vector_type(8))) __bf16 bf16x8;
typedef __attribute__((ext_vector_type(16))) __bf16 bf16x16;
typedef __attribute__((ext_vector_type(4))) short s16x4;
typedef __attribute__((ext_vector_type(16))) float f32x16;
typedef __attribute__((ext_vector_type(8))) float f32x8;
typedef __attribute__((ext_vector_type(4))) float f32x4;
typedef __attribute__((ext_vector_type(2))) float f32x2;
typedef __attribute__((ext_vector_type(16))) unsigned u32x16;
typedef __attribute__((ext_vector_type(4))) unsigned u32x4;
typedef __attribute__((ext_vector_type(2))) unsigned u32x2;
typedef __amdgpu_buffer_rsrc_t srd_t;
#define LAS __attribute__((address_space(3)))
#define GAS __attribute__((address_space(1)))
#define DI __device__ __forceinline__

#define WS_F1 4096
#define WS_F2 8192
#define WS_F3 12288
#define WS_F4 16384
#define WS_F5 20480
#define WS_LN 32768
#define WS_STAMP 131072
#define WS_KV (1u << 20)
#define WS_O (5u << 20)
#define WS_H (7u << 20)
#define WS_U (9u << 20)
#define WS_S (16u << 20)
#define WS_WO (17u << 20)
#define WS_W1 (WS_WO + 524288u)
#define WS_W2 (WS_W1 + 1835008u)
#define WS_END (22u << 20)
#define O_OFF(rt32, ks) ((unsigned)((((rt32) >> 1) * 32 + (ks)) * 2048 + ((rt32) & 1) * 1024))
#define H_OFF(rt32, ks) O_OFF(rt32, ks)
#define U_OFF(rt32, ks) ((unsigned)((((rt32) >> 1) * 8 + (ks) / 14) * 28672 + ((rt32) & 1) * 14336 + ((ks) % 14) * 1024))
#define LA 0
#define LB 65536
#define LC 131072
#define LT (131072 + 28672)
#define P1SLOT 28672
#define LW 143360
#define T_CS (LT)
#define T_A (LT + 3072)
#define T_B (LT + 3584)
#define T_KN (LT)
#define T_CNT (LT + 2048)
#define T_KALL (LT + 2064)
#define T_TAG 131072
#define T_ST2 (LT + 1024)
#define P1LA 3
#define FNU 8
#define CODE_BYTES 31744
#define LEAD 14
#define TCH 6
#define NSTAMP 24
#define SPIN_CAP (1u << 20)

#define MFMA(a, b, c) __builtin_amdgcn_mfma_f32_32x32x16_bf16(a, b, c, 0, 0, 0)
#define SB0() __builtin_amdgcn_sched_barrier(0)
#define WAITV(n) asm volatile("s_waitcnt vmcnt(" #n ")" ::: "memory")
#define BARV(n) asm volatile("s_waitcnt vmcnt(" #n ") lgkmcnt(0)\n\ts_barrier" ::: "memory")
#define BARVN(n) asm volatile("s_waitcnt vmcnt(" #n ")\n\ts_barrier" ::: "memory")
#define SBAR() asm volatile("s_barrier" ::: "memory")
#define BARL() asm volatile("s_waitcnt lgkmcnt(0)\n\ts_barrier" ::: "memory")
#define RLX __ATOMIC_RELAXED, __HIP_MEMORY_SCOPE_AGENT

DI void dma(unsigned lds, unsigned voff, srd_t srd, unsigned soff) {
  asm volatile("s_mov_b32 m0, %0\n\ts_nop 0\n\tbuffer_load_dwordx4 %1, %2, %3 offen lds" ::"s"(lds), "v"(voff), "s"(srd), "s"(soff) : "memory");
}
DI void dma_sc1(unsigned lds, unsigned voff, srd_t srd, unsigned soff) {
  asm volatile("s_mov_b32 m0, %0\n\ts_nop 0\n\tbuffer_load_dwordx4 %1, %2, %3 offen sc1 lds" ::"s"(lds), "v"(voff), "s"(srd), "s"(soff) : "memory");
}
DI void dma_lo32(unsigned lds, unsigned voff, srd_t srd, unsigned soff) {
  asm volatile("s_mov_b32 m0, %0\n\ts_mov_b32 exec_hi, 0\n\tbuffer_load_dwordx4 %1, %2, %3 offen lds\n\ts_mov_b32 exec_hi, -1" ::"s"(lds), "v"(voff), "s"(srd), "s"(soff) : "memory");
}
DI bf16x8 ldsf(unsigned a) { return *(LAS bf16x8*)(size_t)a; }
DI f32x4 lds4(unsigned a) { return *(LAS f32x4*)(size_t)a; }
DI float lds1(unsigned a) { return *(LAS float*)(size_t)a; }
DI bf16x8 trf(unsigned a, unsigned d) {
  s16x4 lo = __builtin_amdgcn_ds_read_tr16_b64_v4i16((LAS s16x4*)(size_t)a);
  s16x4 hi = __builtin_amdgcn_ds_read_tr16_b64_v4i16((LAS s16x4*)(size_t)(a + d));
  return __builtin_bit_cast(bf16x8, __builtin_shufflevector(lo, hi, 0, 1, 2, 3, 4, 5, 6, 7));
}
DI unsigned pk(float a, float b) { bf16x2 v = {(__bf16)a, (__bf16)b}; return __builtin_bit_cast(unsigned, v); }
DI u32x4 pk8(const float* v) { return (u32x4){pk(v[0], v[1]), pk(v[2], v[3]), pk(v[4], v[5]), pk(v[6], v[7])}; }
DI float blo(unsigned v) { return __uint_as_float(v << 16); }
DI float bhi(unsigned v) { return __uint_as_float(v & 0xffff0000u); }
DI float dot_ones(bf16x8 f, float acc) {
  unsigned o1 = 0x3f803f80u;
  asm("" : "+s"(o1));
  const bf16x2 one = __builtin_bit_cast(bf16x2, o1);
  u32x4 u = __builtin_bit_cast(u32x4, f);
#pragma unroll
  for (int i = 0; i < 4; i++) { const unsigned e = u[i]; acc = __builtin_amdgcn_fdot2_f32_bf16(one, __builtin_bit_cast(bf16x2, e), acc, false); }
  return acc;
}
template <int I> DI bf16x8 sub4(const u32x16& v) { return __builtin_bit_cast(bf16x8, __builtin_shufflevector(v, v, 4 * I, 4 * I + 1, 4 * I + 2, 4 * I + 3)); }
DI float xhalf_sum(float v) {
  auto r = __builtin_amdgcn_permlane32_swap(__float_as_uint(v), __float_as_uint(v), false, false);
  return __uint_as_float(r[0]) + __uint_as_float(r[1]);
}
DI float dot_self(bf16x8 f, float acc) {
  u32x4 u = __builtin_bit_cast(u32x4, f);
#pragma unroll
  for (int i = 0; i < 4; i++) { const unsigned e = u[i]; acc = __builtin_amdgcn_fdot2_f32_bf16(__builtin_bit_cast(bf16x2, e), __builtin_bit_cast(bf16x2, e), acc, false); }
  return acc;
}
DI unsigned relu2(unsigned v) { unsigned o; asm("v_pk_max_i16 %0, %1, 0" : "=v"(o) : "v"(v)); return o; }
#define MFMA32(a, b, c) __builtin_amdgcn_mfma_f32_32x32x2f32(a, b, c, 0, 0, 0)
template <int CTRL> DI float dpp_add(float v) { return v + __int_as_float(__builtin_amdgcn_update_dpp(0, __float_as_int(v), CTRL, 0xf, 0xf, true)); }
DI float wave_sum(float v) {
  v = dpp_add<0x121>(v); v = dpp_add<0x122>(v); v = dpp_add<0x124>(v); v = dpp_add<0x128>(v);
  auto a = __builtin_amdgcn_permlane16_swap(__float_as_uint(v), __float_as_uint(v), false, false);
  return xhalf_sum(__uint_as_float(a[0]) + __uint_as_float(a[1]));
}
DI unsigned umax2(unsigned a, unsigned b) { return a > b ? a : b; }
template <int CTRL> DI unsigned dpp_max(unsigned v) { return umax2(v, (unsigned)__builtin_amdgcn_update_dpp((int)v, (int)v, CTRL, 0xf, 0xf, false)); }
DI void wait_tags(const char* line, int n_mask, int l, unsigned tag) {
  GAS unsigned* p = (GAS unsigned*)line + (l & n_mask);
#pragma unroll 1
  for (unsigned sp = 0; sp < SPIN_CAP; ++sp) {
    unsigned f = __hip_atomic_load(p, RLX);
    if (__all(f == tag)) break;
  }
  asm volatile("" ::: "memory");
}

#define EX(n) "v_exp_f32 v" #n ", v" #n "\n\t"
#define LW2 "s_waitcnt lgkmcnt(2)\n\t"
#define MON "s_mov_b64 exec, %[vm]\n\t"
#define MOFF "s_mov_b64 exec, -1\n\t"
#define ATT_OUT "+{v[0:15]}"(SA), "+{v[16:31]}"(SB), "+{v[32:47]}"(OA), "+{v[52:55]}"(Kf), "+{v[56:59]}"(V0), "+{v[60:63]}"(V1)
#define ATT_IN "{v[48:51]}"(Qf), [ix] "v"(sidx), [ka] "v"(kaddr), [va] "v"(vaddr), [vm] "s"(vmask)
#define APARMS f32x16 &SA, f32x16 &SB, f32x16 &OA, u32x4 &Kf, u32x4 &V0, u32x4 &V1, const u32x4 &Qf, unsigned sidx, unsigned kaddr, unsigned vaddr, unsigned long long vmask
template <int N> struct AOff { static constexpr int v = ((N >> 2) & 3) * 16384 + (N & 3) * 1024; };
template <int N, int PA, int PB>
DI void att2(APARMS) {
  asm volatile(
      "s_setprio %[pa]\n\t" EX(0) EX(1) LW2 "v_smfmac_f32_32x32x32_bf16 v[32:47], v[60:63], v[24:31], %[ix]\n\t" MON "ds_read_b128 v[60:63], %[va] offset:%[a1]\n\t" MOFF
      EX(2) EX(3) EX(4) EX(5) LW2 "v_mfma_f32_32x32x16_bf16 v[16:31], v[52:55], v[48:51], 0\n\t" "ds_read_b128 v[52:55], %[ka] offset:%[a2]\n\t"
      EX(6) EX(7) EX(8) EX(9) LW2 "v_smfmac_f32_32x32x32_bf16 v[32:47], v[56:59], v[0:7], %[ix]\n\t" MON "ds_read_b128 v[56:59], %[va] offset:%[a3]\n\t" MOFF
      EX(10) EX(11) EX(12) EX(13) EX(14) EX(15)
      "s_setprio %[pb]\n\t" EX(16) EX(17) LW2 "v_smfmac_f32_32x32x32_bf16 v[32:47], v[60:63], v[8:15], %[ix]\n\t" MON "ds_read_b128 v[60:63], %[va] offset:%[b1]\n\t" MOFF
      EX(18) EX(19) EX(20) EX(21) LW2 "v_mfma_f32_32x32x16_bf16 v[0:15], v[52:55], v[48:51], 0\n\t" "ds_read_b128 v[52:55], %[ka] offset:%[b2]\n\t"
      EX(22) EX(23) EX(24) EX(25) LW2 "v_smfmac_f32_32x32x32_bf16 v[32:47], v[56:59], v[16:23], %[ix]\n\t" MON "ds_read_b128 v[56:59], %[va] offset:%[b3]\n\t" MOFF
      EX(26) EX(27) EX(28) EX(29) EX(30) EX(31)
      : ATT_OUT : ATT_IN, [pa] "n"(PA), [pb] "n"(PB), [a1] "n"(AOff<N>::v + 512), [a2] "n"(AOff<N + 2>::v), [a3] "n"(AOff<N + 1>::v),
      [b1] "n"(AOff<N + 1>::v + 512), [b2] "n"(AOff<N + 3>::v), [b3] "n"(AOff<N + 2>::v)
      : "memory");
}
template <int N>
DI void att_prime(APARMS) {
  asm volatile(
      "s_waitcnt lgkmcnt(0)\n\tds_read_b128 v[52:55], %[ka] offset:%[k0]\n\ts_waitcnt lgkmcnt(0)\n\t"
      "v_mfma_f32_32x32x16_bf16 v[0:15], v[52:55], v[48:51], 0\n\t"
      "ds_read_b128 v[52:55], %[ka] offset:%[k1]\n\t" MON "ds_read_b128 v[56:59], %[va] offset:%[k0]\n\t" MOFF
      "s_nop 7\n\ts_nop 3\n\t"
      : ATT_OUT : ATT_IN, [k0] "n"(AOff<N>::v), [k1] "n"(AOff<N + 1>::v)
      : "memory");
}
#define ATT_MAIN_ASM \
  "s_setprio 3\n" \
  "v_exp_f32 v0, v0\n" \
  "v_exp_f32 v1, v1\n" \
  "v_smfmac_f32_32x32x32_bf16 v[32:47], v[60:63], v[24:31], %[ix]\n" \
  "s_mov_b64 exec, %[vm]\n" \
  "ds_read_b128 v[60:63], %[va] offset:16896\n" \
  "s_mov_b64 exec, 1\n" \
  "s_waitcnt vmcnt(2)\n" \
  "ds_add_u32 %[ca], %[ix]\n" \
  "s_mov_b64 exec, -1\n" \
  "v_exp_f32 v2, v2\n" \
  "v_exp_f32 v3, v3\n" \
  "v_exp_f32 v4, v4\n" \
  "v_exp_f32 v5, v5\n" \
  "s_waitcnt lgkmcnt(3)\n" \
  "v_mfma_f32_32x32x16_bf16 v[16:31], v[52:55], v[48:51], 0\n" \
  "ds_read_b128 v[52:55], %[ka] offset:18432\n" \
  "v_exp_f32 v6, v6\n" \
  "v_exp_f32 v7, v7\n" \
  "v_exp_f32 v8, v8\n" \
  "v_exp_f32 v9, v9\n" \
  "s_waitcnt lgkmcnt(3)\n" \
  "v_smfmac_f32_32x32x32_bf16 v[32:47], v[56:59], v[0:7], %[ix]\n" \
  "s_mov_b64 exec, %[vm]\n" \
  "ds_read_b128 v[56:59], %[va] offset:17408\n" \
  "s_mov_b64 exec, -1\n" \
  "v_exp_f32 v10, v10\n" \
  "v_exp_f32 v11, v11\n" \
  "v_exp_f32 v12, v12\n" \
  "v_exp_f32 v13, v13\n" \
  "v_exp_f32 v14, v14\n" \
  "v_exp_f32 v15, v15\n" \
  "s_setprio 2\n" \
  "v_exp_f32 v16, v16\n" \
  "v_exp_f32 v17, v17\n" \
  "s_waitcnt lgkmcnt(3)\n" \
  "v_smfmac_f32_32x32x32_bf16 v[32:47], v[60:63], v[8:15], %[ix]\n" \
  "s_mov_b64 exec, %[vm]\n" \
  "ds_read_b128 v[60:63], %[va] offset:17920\n" \
  "s_mov_b64 exec, -1\n" \
  "v_exp_f32 v18, v18\n" \
  "v_exp_f32 v19, v19\n" \
  "v_exp_f32 v20, v20\n" \
  "v_exp_f32 v21, v21\n" \
  "s_waitcnt lgkmcnt(2)\n" \
  "v_mfma_f32_32x32x16_bf16 v[0:15], v[52:55], v[48:51], 0\n" \
  "ds_read_b128 v[52:55], %[ka] offset:19456\n" \
  "v_exp_f32 v22, v22\n" \
  "v_exp_f32 v23, v23\n" \
  "v_exp_f32 v24, v24\n" \
  "v_exp_f32 v25, v25\n" \
  "s_waitcnt lgkmcnt(2)\n" \
  "v_smfmac_f32_32x32x32_bf16 v[32:47], v[56:59], v[16:23], %[ix]\n" \
  "s_mov_b64 exec, %[vm]\n" \
  "ds_read_b128 v[56:59], %[va] offset:18432\n" \
  "s_mov_b64 exec, -1\n" \
  "ds_read_b32 %[ct], %[ca]\n" \
  "v_exp_f32 v26, v26\n" \
  "v_exp_f32 v27, v27\n" \
  "v_exp_f32 v28, v28\n" \
  "v_exp_f32 v29, v29\n" \
  "v_exp_f32 v30, v30\n" \
  "v_exp_f32 v31, v31\n" \
  "s_setprio 1\n" \
  "v_exp_f32 v0, v0\n" \
  "v_exp_f32 v1, v1\n" \
  "s_waitcnt lgkmcnt(3)\n" \
  "v_smfmac_f32_32x32x32_bf16 v[32:47], v[60:63], v[24:31], %[ix]\n" \
  "s_mov_b64 exec, %[vm]\n" \
  "ds_read_b128 v[60:63], %[va] offset:18944\n" \
  "s_mov_b64 exec, -1\n" \
  "v_exp_f32 v2, v2\n" \
  "v_exp_f32 v3, v3\n" \
  "s_waitcnt lgkmcnt(1)\n" \
  "v_readfirstlane_b32 %[t1], %[ct]\n" \
  "s_cmp_ge_u32 %[t1], 227188\n" \
  "s_cbranch_scc0 Lspin1_%=\n" \
  "Lback1_%=:\n" \
  "v_exp_f32 v4, v4\n" \
  "v_exp_f32 v5, v5\n" \
  "v_mfma_f32_32x32x16_bf16 v[16:31], v[52:55], v[48:51], 0\n" \
  "ds_read_b128 v[52:55], %[ka] offset:32768\n" \
  "v_exp_f32 v6, v6\n" \
  "v_exp_f32 v7, v7\n" \
  "v_exp_f32 v8, v8\n" \
  "v_exp_f32 v9, v9\n" \
  "v_smfmac_f32_32x32x32_bf16 v[32:47], v[56:59], v[0:7], %[ix]\n" \
  "s_mov_b64 exec, %[vm]\n" \
  "ds_read_b128 v[56:59], %[va] offset:19456\n" \
  "s_mov_b64 exec, -1\n" \
  "v_exp_f32 v10, v10\n" \
  "v_exp_f32 v11, v11\n" \
  "v_exp_f32 v12, v12\n" \
  "s_add_u32 %[rot], %[rot], 8192\n" \
  "s_and_b32 %[rot], %[rot], 0x1e000\n" \
  "s_add_u32 m0, %[kvd], 0\n" \
  "s_add_u32 %[t0], %[rot], %[kvs]\n" \
  "buffer_load_dwordx4 %[ka], %[sw], %[t0] offen sc1 lds\n" \
  "v_exp_f32 v13, v13\n" \
  "v_exp_f32 v14, v14\n" \
  "v_exp_f32 v15, v15\n" \
  "s_setprio 0\n" \
  "v_exp_f32 v16, v16\n" \
  "v_exp_f32 v17, v17\n" \
  "s_waitcnt lgkmcnt(2)\n" \
  "v_smfmac_f32_32x32x32_bf16 v[32:47], v[60:63], v[8:15], %[ix]\n" \
  "s_mov_b64 exec, %[vm]\n" \
  "ds_read_b128 v[60:63], %[va] offset:19968\n" \
  "s_mov_b64 exec, -1\n" \
  "v_exp_f32 v18, v18\n" \
  "v_exp_f32 v19, v19\n" \
  "v_exp_f32 v20, v20\n" \
  "v_exp_f32 v21, v21\n" \
  "s_waitcnt lgkmcnt(2)\n" \
  "v_mfma_f32_32x32x16_bf16 v[0:15], v[52:55], v[48:51], 0\n" \
  "ds_read_b128 v[52:55], %[ka] offset:33792\n" \
  "v_exp_f32 v22, v22\n" \
  "v_exp_f32 v23, v23\n" \
  "v_exp_f32 v24, v24\n" \
  "v_exp_f32 v25, v25\n" \
  "s_waitcnt lgkmcnt(2)\n" \
  "v_smfmac_f32_32x32x32_bf16 v[32:47], v[56:59], v[16:23], %[ix]\n" \
  "s_mov_b64 exec, %[vm]\n" \
  "ds_read_b128 v[56:59], %[va] offset:32768\n" \
  "s_mov_b64 exec, -1\n" \
  "v_exp_f32 v26, v26\n" \
  "v_exp_f32 v27, v27\n" \
  "v_exp_f32 v28, v28\n" \
  "s_add_u32 m0, %[kvd], 0\n" \
  "s_add_u32 %[t0], %[rot], %[kvs]\n" \
  "buffer_load_dwordx4 %[ka], %[sw], %[t0] offen offset:1024 sc1 lds\n" \
  "v_exp_f32 v29, v29\n" \
  "v_exp_f32 v30, v30\n" \
  "v_exp_f32 v31, v31\n" \
  "s_setprio 3\n" \
  "v_exp_f32 v0, v0\n" \
  "v_exp_f32 v1, v1\n" \
  "s_waitcnt lgkmcnt(2)\n" \
  "v_smfmac_f32_32x32x32_bf16 v[32:47], v[60:63], v[24:31], %[ix]\n" \
  "s_mov_b64 exec, %[vm]\n" \
  "ds_read_b128 v[60:63], %[va] offset:33280\n" \
  "s_mov_b64 exec, 1\n" \
  "s_waitcnt vmcnt(2)\n" \
  "ds_add_u32 %[ca], %[ix]\n" \
  "s_mov_b64 exec, -1\n" \
  "v_exp_f32 v2, v2\n" \
  "v_exp_f32 v3, v3\n" \
  "v_exp_f32 v4, v4\n" \
  "v_exp_f32 v5, v5\n" \
  "s_waitcnt lgkmcnt(3)\n" \
  "v_mfma_f32_32x32x16_bf16 v[16:31], v[52:55], v[48:51], 0\n" \
  "ds_read_b128 v[52:55], %[ka] offset:34816\n" \
  "v_exp_f32 v6, v6\n" \
  "v_exp_f32 v7, v7\n" \
  "v_exp_f32 v8, v8\n" \
  "v_exp_f32 v9, v9\n" \
  "s_waitcnt lgkmcnt(3)\n" \
  "v_smfmac_f32_32x32x32_bf16 v[32:47], v[56:59], v[0:7], %[ix]\n" \
  "s_mov_b64 exec, %[vm]\n" \
  "ds_read_b128 v[56:59], %[va] offset:33792\n" \
  "s_mov_b64 exec, -1\n" \
  "v_exp_f32 v10, v10\n" \
  "v_exp_f32 v11, v11\n" \
  "v_exp_f32 v12, v12\n" \
  "v_exp_f32 v13, v13\n" \
  "v_exp_f32 v14, v14\n" \
  "v_exp_f32 v15, v15\n" \
  "s_setprio 2\n" \
  "v_exp_f32 v16, v16\n" \
  "v_exp_f32 v17, v17\n" \
  "s_waitcnt lgkmcnt(3)\n" \
  "v_smfmac_f32_32x32x32_bf16 v[32:47], v[60:63], v[8:15], %[ix]\n" \
  "s_mov_b64 exec, %[vm]\n" \
  "ds_read_b128 v[60:63], %[va] offset:34304\n" \
  "s_mov_b64 exec, -1\n" \
  "v_exp_f32 v18, v18\n" \
  "v_exp_f32 v19, v19\n" \
  "buffer_load_dwordx4 v[236:239], %[ka], %[sw], %[f5] offen sc1\n" \
  "v_exp_f32 v20, v20\n" \
  "v_exp_f32 v21, v21\n" \
  "s_waitcnt lgkmcnt(2)\n" \
  "v_mfma_f32_32x32x16_bf16 v[0:15], v[52:55], v[48:51], 0\n" \
  "ds_read_b128 v[52:55], %[ka] offset:35840\n" \
  "v_exp_f32 v22, v22\n" \
  "v_exp_f32 v23, v23\n" \
  "v_exp_f32 v24, v24\n" \
  "v_exp_f32 v25, v25\n" \
  "s_waitcnt lgkmcnt(2)\n" \
  "v_smfmac_f32_32x32x32_bf16 v[32:47], v[56:59], v[16:23], %[ix]\n" \
  "s_mov_b64 exec, %[vm]\n" \
  "ds_read_b128 v[56:59], %[va] offset:34816\n" \
  "s_mov_b64 exec, -1\n" \
  "ds_read_b32 %[ct], %[ca]\n" \
  "v_exp_f32 v26, v26\n" \
  "v_exp_f32 v27, v27\n" \
  "v_exp_f32 v28, v28\n" \
  "v_exp_f32 v29, v29\n" \
  "v_exp_f32 v30, v30\n" \
  "v_exp_f32 v31, v31\n" \
  "s_setprio 1\n" \
  "v_exp_f32 v0, v0\n" \
  "v_exp_f32 v1, v1\n" \
  "s_waitcnt lgkmcnt(3)\n" \
  "v_smfmac_f32_32x32x32_bf16 v[32:47], v[60:63], v[24:31], %[ix]\n" \
  "s_mov_b64 exec, %[vm]\n" \
  "ds_read_b128 v[60:63], %[va] offset:35328\n" \
  "s_mov_b64 exec, -1\n" \
  "v_exp_f32 v2, v2\n" \
  "v_exp_f32 v3, v3\n" \
  "s_waitcnt lgkmcnt(1)\n" \
  "v_readfirstlane_b32 %[t1], %[ct]\n" \
  "s_cmp_ge_u32 %[t1], 454376\n" \
  "s_cbranch_scc0 Lspin2_%=\n" \
  "Lback2_%=:\n" \
  "v_exp_f32 v4, v4\n" \
  "v_exp_f32 v5, v5\n" \
  "v_mfma_f32_32x32x16_bf16 v[16:31], v[52:55], v[48:51], 0\n" \
  "ds_read_b128 v[52:55], %[ka] offset:49152\n" \
  "v_exp_f32 v6, v6\n" \
  "v_exp_f32 v7, v7\n" \
  "v_exp_f32 v8, v8\n" \
  "v_exp_f32 v9, v9\n" \
  "v_smfmac_f32_32x32x32_bf16 v[32:47], v[56:59], v[0:7], %[ix]\n" \
  "s_mov_b64 exec, %[vm]\n" \
  "ds_read_b128 v[56:59], %[va] offset:35840\n" \
  "s_mov_b64 exec, -1\n" \
  "v_exp_f32 v10, v10\n" \
  "v_exp_f32 v11, v11\n" \
  "v_exp_f32 v12, v12\n" \
  "s_add_u32 %[rot], %[rot], 8192\n" \
  "s_and_b32 %[rot], %[rot], 0x1e000\n" \
  "s_add_u32 m0, %[kvd], 16384\n" \
  "s_add_u32 %[t0], %[rot], %[kvs]\n" \
  "buffer_load_dwordx4 %[ka], %[sw], %[t0] offen sc1 lds\n" \
  "v_exp_f32 v13, v13\n" \
  "v_exp_f32 v14, v14\n" \
  "v_exp_f32 v15, v15\n" \
  "s_setprio 0\n" \
  "v_exp_f32 v16, v16\n" \
  "v_exp_f32 v17, v17\n" \
  "s_waitcnt lgkmcnt(2)\n" \
  "v_smfmac_f32_32x32x32_bf16 v[32:47], v[60:63], v[8:15], %[ix]\n" \
  "s_mov_b64 exec, %[vm]\n" \
  "ds_read_b128 v[60:63], %[va] offset:36352\n" \
  "s_mov_b64 exec, -1\n" \
  "v_exp_f32 v18, v18\n" \
  "v_exp_f32 v19, v19\n" \
  "v_exp_f32 v20, v20\n" \
  "v_exp_f32 v21, v21\n" \
  "s_waitcnt lgkmcnt(2)\n" \
  "v_mfma_f32_32x32x16_bf16 v[0:15], v[52:55], v[48:51], 0\n" \
  "ds_read_b128 v[52:55], %[ka] offset:50176\n" \
  "v_exp_f32 v22, v22\n" \
  "v_exp_f32 v23, v23\n" \
  "v_exp_f32 v24, v24\n" \
  "v_exp_f32 v25, v25\n" \
  "s_waitcnt lgkmcnt(2)\n" \
  "v_smfmac_f32_32x32x32_bf16 v[32:47], v[56:59], v[16:23], %[ix]\n" \
  "s_mov_b64 exec, %[vm]\n" \
  "ds_read_b128 v[56:59], %[va] offset:49152\n" \
  "s_mov_b64 exec, -1\n" \
  "v_exp_f32 v26, v26\n" \
  "v_exp_f32 v27, v27\n" \
  "v_exp_f32 v28, v28\n" \
  "s_add_u32 m0, %[kvd], 16384\n" \
  "s_add_u32 %[t0], %[rot], %[kvs]\n" \
  "buffer_load_dwordx4 %[ka], %[sw], %[t0] offen offset:1024 sc1 lds\n" \
  "v_exp_f32 v29, v29\n" \
  "v_exp_f32 v30, v30\n" \
  "v_exp_f32 v31, v31\n" \
  "s_setprio 3\n" \
  "v_exp_f32 v0, v0\n" \
  "v_exp_f32 v1, v1\n" \
  "s_waitcnt lgkmcnt(2)\n" \
  "v_smfmac_f32_32x32x32_bf16 v[32:47], v[60:63], v[24:31], %[ix]\n" \
  "s_mov_b64 exec, %[vm]\n" \
  "ds_read_b128 v[60:63], %[va] offset:49664\n" \
  "s_mov_b64 exec, 1\n" \
  "s_waitcnt vmcnt(3)\n" \
  "ds_add_u32 %[ca], %[ix]\n" \
  "s_mov_b64 exec, -1\n" \
  "v_exp_f32 v2, v2\n" \
  "v_exp_f32 v3, v3\n" \
  "s_waitcnt vmcnt(2)\n" \
  "v_cmp_ne_u32 vcc, %[tag], v236\n" \
  "v_cmp_ne_u32 %[p0], %[tag], v237\n" \
  "s_or_b64 vcc, vcc, %[p0]\n" \
  "v_cmp_ne_u32 %[p0], %[tag], v238\n" \
  "s_or_b64 vcc, vcc, %[p0]\n" \
  "v_cmp_ne_u32 %[p0], %[tag], v239\n" \
  "s_or_b64 vcc, vcc, %[p0]\n" \
  "s_cbranch_scc1 Lwsp_%=\n" \
  "Lwok_%=:\n" \
  "buffer_load_dwordx4 v[64:67], %[ka], %[sw], %[w1t] offen offset:0 sc1\n" \
  "v_exp_f32 v4, v4\n" \
  "v_exp_f32 v5, v5\n" \
  "s_waitcnt lgkmcnt(3)\n" \
  "v_mfma_f32_32x32x16_bf16 v[16:31], v[52:55], v[48:51], 0\n" \
  "ds_read_b128 v[52:55], %[ka] offset:51200\n" \
  "v_exp_f32 v6, v6\n" \
  "v_exp_f32 v7, v7\n" \
  "v_exp_f32 v8, v8\n" \
  "v_exp_f32 v9, v9\n" \
  "s_waitcnt lgkmcnt(3)\n" \
  "v_smfmac_f32_32x32x32_bf16 v[32:47], v[56:59], v[0:7], %[ix]\n" \
  "s_mov_b64 exec, %[vm]\n" \
  "ds_read_b128 v[56:59], %[va] offset:50176\n" \
  "s_mov_b64 exec, -1\n" \
  "v_exp_f32 v10, v10\n" \
  "v_exp_f32 v11, v11\n" \
  "v_exp_f32 v12, v12\n" \
  "buffer_load_dwordx4 v[68:71], %[ka], %[sw], %[w1t] offen offset:1024 sc1\n" \
  "v_exp_f32 v13, v13\n" \
  "v_exp_f32 v14, v14\n" \
  "v_exp_f32 v15, v15\n" \
  "s_setprio 2\n" \
  "v_exp_f32 v16, v16\n" \
  "v_exp_f32 v17, v17\n" \
  "s_waitcnt lgkmcnt(3)\n" \
  "v_smfmac_f32_32x32x32_bf16 v[32:47], v[60:63], v[8:15], %[ix]\n" \
  "s_mov_b64 exec, %[vm]\n" \
  "ds_read_b128 v[60:63], %[va] offset:50688\n" \
  "s_mov_b64 exec, -1\n" \
  "v_exp_f32 v18, v18\n" \
  "v_exp_f32 v19, v19\n" \
  "buffer_load_dwordx4 v[72:75], %[ka], %[sw], %[w1t] offen offset:2048 sc1\n" \
  "v_exp_f32 v20, v20\n" \
  "v_exp_f32 v21, v21\n" \
  "s_waitcnt lgkmcnt(2)\n" \
  "v_mfma_f32_32x32x16_bf16 v[0:15], v[52:55], v[48:51], 0\n" \
  "ds_read_b128 v[52:55], %[ka] offset:52224\n" \
  "v_exp_f32 v22, v22\n" \
  "v_exp_f32 v23, v23\n" \
  "v_exp_f32 v24, v24\n" \
  "v_exp_f32 v25, v25\n" \
  "s_waitcnt lgkmcnt(2)\n" \
  "v_smfmac_f32_32x32x32_bf16 v[32:47], v[56:59], v[16:23], %[ix]\n" \
  "s_mov_b64 exec, %[vm]\n" \
  "ds_read_b128 v[56:59], %[va] offset:51200\n" \
  "s_mov_b64 exec, -1\n" \
  "ds_read_b32 %[ct], %[ca]\n" \
  "v_exp_f32 v26, v26\n" \
  "v_exp_f32 v27, v27\n" \
  "v_exp_f32 v28, v28\n" \
  "buffer_load_dwordx4 v[76:79], %[ka], %[sw], %[w1t] offen offset:3072 sc1\n" \
  "v_exp_f32 v29, v29\n" \
  "v_exp_f32 v30, v30\n" \
  "v_exp_f32 v31, v31\n" \
  "s_setprio 1\n" \
  "v_exp_f32 v0, v0\n" \
  "v_exp_f32 v1, v1\n" \
  "s_waitcnt lgkmcnt(3)\n" \
  "v_smfmac_f32_32x32x32_bf16 v[32:47], v[60:63], v[24:31], %[ix]\n" \
  "s_mov_b64 exec, %[vm]\n" \
  "ds_read_b128 v[60:63], %[va] offset:51712\n" \
  "s_mov_b64 exec, -1\n" \
  "v_exp_f32 v2, v2\n" \
  "v_exp_f32 v3, v3\n" \
  "s_waitcnt lgkmcnt(1)\n" \
  "v_readfirstlane_b32 %[t1], %[ct]\n" \
  "s_cmp_ge_u32 %[t1], 681564\n" \
  "s_cbranch_scc0 Lspin3_%=\n" \
  "Lback3_%=:\n" \
  "s_add_u32 %[w1t], %[w1t], 4096\n" \
  "buffer_load_dwordx4 v[80:83], %[ka], %[sw], %[w1t] offen offset:0 sc1\n" \
  "v_exp_f32 v4, v4\n" \
  "v_exp_f32 v5, v5\n" \
  "v_mfma_f32_32x32x16_bf16 v[16:31], v[52:55], v[48:51], 0\n" \
  "ds_read_b128 v[52:55], %[ka] offset:0\n" \
  "v_exp_f32 v6, v6\n" \
  "v_exp_f32 v7, v7\n" \
  "v_exp_f32 v8, v8\n" \
  "v_exp_f32 v9, v9\n" \
  "v_smfmac_f32_32x32x32_bf16 v[32:47], v[56:59], v[0:7], %[ix]\n" \
  "s_mov_b64 exec, %[vm]\n" \
  "ds_read_b128 v[56:59], %[va] offset:52224\n" \
  "s_mov_b64 exec, -1\n" \
  "v_exp_f32 v10, v10\n" \
  "v_exp_f32 v11, v11\n" \
  "v_exp_f32 v12, v12\n" \
  "s_add_u32 %[rot], %[rot], 8192\n" \
  "s_and_b32 %[rot], %[rot], 0x1e000\n" \
  "s_add_u32 m0, %[kvd], 32768\n" \
  "s_add_u32 %[t0], %[rot], %[kvs]\n" \
  "buffer_load_dwordx4 %[ka], %[sw], %[t0] offen sc1 lds\n" \
  "v_exp_f32 v13, v13\n" \
  "v_exp_f32 v14, v14\n" \
  "v_exp_f32 v15, v15\n" \
  "s_setprio 0\n" \
  "v_exp_f32 v16, v16\n" \
  "v_exp_f32 v17, v17\n" \
  "s_waitcnt lgkmcnt(2)\n" \
  "v_smfmac_f32_32x32x32_bf16 v[32:47], v[60:63], v[8:15], %[ix]\n" \
  "s_mov_b64 exec, %[vm]\n" \
  "ds_read_b128 v[60:63], %[va] offset:52736\n" \
  "s_mov_b64 exec, -1\n" \
  "v_exp_f32 v18, v18\n" \
  "v_exp_f32 v19, v19\n" \
  "buffer_load_dwordx4 v[84:87], %[ka], %[sw], %[w1t] offen offset:1024 sc1\n" \
  "v_exp_f32 v20, v20\n" \
  "v_exp_f32 v21, v21\n" \
  "s_waitcnt lgkmcnt(2)\n" \
  "v_mfma_f32_32x32x16_bf16 v[0:15], v[52:55], v[48:51], 0\n" \
  "ds_read_b128 v[52:55], %[ka] offset:1024\n" \
  "v_exp_f32 v22, v22\n" \
  "v_exp_f32 v23, v23\n" \
  "v_exp_f32 v24, v24\n" \
  "v_exp_f32 v25, v25\n" \
  "s_waitcnt lgkmcnt(2)\n" \
  "v_smfmac_f32_32x32x32_bf16 v[32:47], v[56:59], v[16:23], %[ix]\n" \
  "s_mov_b64 exec, %[vm]\n" \
  "ds_read_b128 v[56:59], %[va] offset:0\n" \
  "s_mov_b64 exec, -1\n" \
  "v_exp_f32 v26, v26\n" \
  "v_exp_f32 v27, v27\n" \
  "v_exp_f32 v28, v28\n" \
  "s_add_u32 m0, %[kvd], 32768\n" \
  "s_add_u32 %[t0], %[rot], %[kvs]\n" \
  "buffer_load_dwordx4 %[ka], %[sw], %[t0] offen offset:1024 sc1 lds\n" \
  "v_exp_f32 v29, v29\n" \
  "v_exp_f32 v30, v30\n" \
  "v_exp_f32 v31, v31\n" \
  "s_setprio 3\n" \
  "v_exp_f32 v0, v0\n" \
  "v_exp_f32 v1, v1\n" \
  "s_waitcnt lgkmcnt(2)\n" \
  "v_smfmac_f32_32x32x32_bf16 v[32:47], v[60:63], v[24:31], %[ix]\n" \
  "s_mov_b64 exec, %[vm]\n" \
  "ds_read_b128 v[60:63], %[va] offset:512\n" \
  "s_mov_b64 exec, 1\n" \
  "s_waitcnt vmcnt(8)\n" \
  "ds_add_u32 %[ca], %[ix]\n" \
  "s_mov_b64 exec, -1\n" \
  "v_exp_f32 v2, v2\n" \
  "v_exp_f32 v3, v3\n" \
  "buffer_load_dwordx4 v[88:91], %[ka], %[sw], %[w1t] offen offset:2048 sc1\n" \
  "v_exp_f32 v4, v4\n" \
  "v_exp_f32 v5, v5\n" \
  "s_waitcnt lgkmcnt(3)\n" \
  "v_mfma_f32_32x32x16_bf16 v[16:31], v[52:55], v[48:51], 0\n" \
  "ds_read_b128 v[52:55], %[ka] offset:2048\n" \
  "v_exp_f32 v6, v6\n" \
  "v_exp_f32 v7, v7\n" \
  "v_exp_f32 v8, v8\n" \
  "v_exp_f32 v9, v9\n" \
  "s_waitcnt lgkmcnt(3)\n" \
  "v_smfmac_f32_32x32x32_bf16 v[32:47], v[56:59], v[0:7], %[ix]\n" \
  "s_mov_b64 exec, %[vm]\n" \
  "ds_read_b128 v[56:59], %[va] offset:1024\n" \
  "s_mov_b64 exec, -1\n" \
  "v_exp_f32 v10, v10\n" \
  "v_exp_f32 v11, v11\n" \
  "v_exp_f32 v12, v12\n" \
  "buffer_load_dwordx4 v[92:95], %[ka], %[sw], %[w1t] offen offset:3072 sc1\n" \
  "v_exp_f32 v13, v13\n" \
  "v_exp_f32 v14, v14\n" \
  "v_exp_f32 v15, v15\n" \
  "s_setprio 2\n" \
  "v_exp_f32 v16, v16\n" \
  "v_exp_f32 v17, v17\n" \
  "s_waitcnt lgkmcnt(3)\n" \
  "v_smfmac_f32_32x32x32_bf16 v[32:47], v[60:63], v[8:15], %[ix]\n" \
  "s_mov_b64 exec, %[vm]\n" \
  "ds_read_b128 v[60:63], %[va] offset:1536\n" \
  "s_mov_b64 exec, -1\n" \
  "v_exp_f32 v18, v18\n" \
  "v_exp_f32 v19, v19\n" \
  "s_add_u32 %[w1t], %[w1t], 4096\n" \
  "buffer_load_dwordx4 v[96:99], %[ka], %[sw], %[w1t] offen offset:0 sc1\n" \
  "v_exp_f32 v20, v20\n" \
  "v_exp_f32 v21, v21\n" \
  "s_waitcnt lgkmcnt(2)\n" \
  "v_mfma_f32_32x32x16_bf16 v[0:15], v[52:55], v[48:51], 0\n" \
  "ds_read_b128 v[52:55], %[ka] offset:3072\n" \
  "v_exp_f32 v22, v22\n" \
  "v_exp_f32 v23, v23\n" \
  "v_exp_f32 v24, v24\n" \
  "v_exp_f32 v25, v25\n" \
  "s_waitcnt lgkmcnt(2)\n" \
  "v_smfmac_f32_32x32x32_bf16 v[32:47], v[56:59], v[16:23], %[ix]\n" \
  "s_mov_b64 exec, %[vm]\n" \
  "ds_read_b128 v[56:59], %[va] offset:2048\n" \
  "s_mov_b64 exec, -1\n" \
  "ds_read_b32 %[ct], %[ca]\n" \
  "v_exp_f32 v26, v26\n" \
  "v_exp_f32 v27, v27\n" \
  "v_exp_f32 v28, v28\n" \
  "buffer_load_dwordx4 v[100:103], %[ka], %[sw], %[w1t] offen offset:1024 sc1\n" \
  "v_exp_f32 v29, v29\n" \
  "v_exp_f32 v30, v30\n" \
  "v_exp_f32 v31, v31\n" \
  "s_setprio 1\n" \
  "v_exp_f32 v0, v0\n" \
  "v_exp_f32 v1, v1\n" \
  "s_waitcnt lgkmcnt(3)\n" \
  "v_smfmac_f32_32x32x32_bf16 v[32:47], v[60:63], v[24:31], %[ix]\n" \
  "s_mov_b64 exec, %[vm]\n" \
  "ds_read_b128 v[60:63], %[va] offset:2560\n" \
  "s_mov_b64 exec, -1\n" \
  "v_exp_f32 v2, v2\n" \
  "v_exp_f32 v3, v3\n" \
  "s_waitcnt lgkmcnt(1)\n" \
  "v_readfirstlane_b32 %[t1], %[ct]\n" \
  "s_cmp_ge_u32 %[t1], 908752\n" \
  "s_cbranch_scc0 Lspin4_%=\n" \
  "Lback4_%=:\n" \
  "buffer_load_dwordx4 v[104:107], %[ka], %[sw], %[w1t] offen offset:2048 sc1\n" \
  "v_exp_f32 v4, v4\n" \
  "v_exp_f32 v5, v5\n" \
  "v_mfma_f32_32x32x16_bf16 v[16:31], v[52:55], v[48:51], 0\n" \
  "ds_read_b128 v[52:55], %[ka] offset:16384\n" \
  "v_exp_f32 v6, v6\n" \
  "v_exp_f32 v7, v7\n" \
  "v_exp_f32 v8, v8\n" \
  "v_exp_f32 v9, v9\n" \
  "v_smfmac_f32_32x32x32_bf16 v[32:47], v[56:59], v[0:7], %[ix]\n" \
  "s_mov_b64 exec, %[vm]\n" \
  "ds_read_b128 v[56:59], %[va] offset:3072\n" \
  "s_mov_b64 exec, -1\n" \
  "v_exp_f32 v10, v10\n" \
  "v_exp_f32 v11, v11\n" \
  "v_exp_f32 v12, v12\n" \
  "s_add_u32 %[rot], %[rot], 8192\n" \
  "s_and_b32 %[rot], %[rot], 0x1e000\n" \
  "s_add_u32 m0, %[kvd], 49152\n" \
  "s_add_u32 %[t0], %[rot], %[kvs]\n" \
  "buffer_load_dwordx4 %[ka], %[sw], %[t0] offen sc1 lds\n" \
  "v_exp_f32 v13, v13\n" \
  "v_exp_f32 v14, v14\n" \
  "v_exp_f32 v15, v15\n" \
  "s_setprio 0\n" \
  "v_exp_f32 v16, v16\n" \
  "v_exp_f32 v17, v17\n" \
  "s_waitcnt lgkmcnt(2)\n" \
  "v_smfmac_f32_32x32x32_bf16 v[32:47], v[60:63], v[8:15], %[ix]\n" \
  "s_mov_b64 exec, %[vm]\n" \
  "ds_read_b128 v[60:63], %[va] offset:3584\n" \
  "s_mov_b64 exec, -1\n" \
  "v_exp_f32 v18, v18\n" \
  "v_exp_f32 v19, v19\n" \
  "buffer_load_dwordx4 v[108:111], %[ka], %[sw], %[w1t] offen offset:3072 sc1\n" \
  "v_exp_f32 v20, v20\n" \
  "v_exp_f32 v21, v21\n" \
  "s_waitcnt lgkmcnt(2)\n" \
  "v_mfma_f32_32x32x16_bf16 v[0:15], v[52:55], v[48:51], 0\n" \
  "ds_read_b128 v[52:55], %[ka] offset:17408\n" \
  "v_exp_f32 v22, v22\n" \
  "v_exp_f32 v23, v23\n" \
  "v_exp_f32 v24, v24\n" \
  "v_exp_f32 v25, v25\n" \
  "s_waitcnt lgkmcnt(2)\n" \
  "v_smfmac_f32_32x32x32_bf16 v[32:47], v[56:59], v[16:23], %[ix]\n" \
  "s_mov_b64 exec, %[vm]\n" \
  "ds_read_b128 v[56:59], %[va] offset:16384\n" \
  "s_mov_b64 exec, -1\n" \
  "v_exp_f32 v26, v26\n" \
  "v_exp_f32 v27, v27\n" \
  "v_exp_f32 v28, v28\n" \
  "s_add_u32 m0, %[kvd], 49152\n" \
  "s_add_u32 %[t0], %[rot], %[kvs]\n" \
  "buffer_load_dwordx4 %[ka], %[sw], %[t0] offen offset:1024 sc1 lds\n" \
  "v_exp_f32 v29, v29\n" \
  "v_exp_f32 v30, v30\n" \
  "v_exp_f32 v31, v31\n" \
  "s_setprio 3\n" \
  "v_exp_f32 v0, v0\n" \
  "v_exp_f32 v1, v1\n" \
  "s_waitcnt lgkmcnt(2)\n" \
  "v_smfmac_f32_32x32x32_bf16 v[32:47], v[60:63], v[24:31], %[ix]\n" \
  "s_mov_b64 exec, %[vm]\n" \
  "ds_read_b128 v[60:63], %[va] offset:16896\n" \
  "s_mov_b64 exec, 1\n" \
  "s_waitcnt vmcnt(8)\n" \
  "ds_add_u32 %[ca], %[ix]\n" \
  "s_mov_b64 exec, -1\n" \
  "v_exp_f32 v2, v2\n" \
  "v_exp_f32 v3, v3\n" \
  "s_add_u32 %[w1t], %[w1t], 4096\n" \
  "buffer_load_dwordx4 v[112:115], %[ka], %[sw], %[w1t] offen offset:0 sc1\n" \
  "v_exp_f32 v4, v4\n" \
  "v_exp_f32 v5, v5\n" \
  "s_waitcnt lgkmcnt(3)\n" \
  "v_mfma_f32_32x32x16_bf16 v[16:31], v[52:55], v[48:51], 0\n" \
  "ds_read_b128 v[52:55], %[ka] offset:18432\n" \
  "v_exp_f32 v6, v6\n" \
  "v_exp_f32 v7, v7\n" \
  "v_exp_f32 v8, v8\n" \
  "v_exp_f32 v9, v9\n" \
  "s_waitcnt lgkmcnt(3)\n" \
  "v_smfmac_f32_32x32x32_bf16 v[32:47], v[56:59], v[0:7], %[ix]\n" \
  "s_mov_b64 exec, %[vm]\n" \
  "ds_read_b128 v[56:59], %[va] offset:17408\n" \
  "s_mov_b64 exec, -1\n" \
  "v_exp_f32 v10, v10\n" \
  "v_exp_f32 v11, v11\n" \
  "v_exp_f32 v12, v12\n" \
  "v_exp_f32 v13, v13\n" \
  "v_exp_f32 v14, v14\n" \
  "v_exp_f32 v15, v15\n" \
  "s_setprio 2\n" \
  "v_exp_f32 v16, v16\n" \
  "v_exp_f32 v17, v17\n" \
  "s_waitcnt lgkmcnt(3)\n" \
  "v_smfmac_f32_32x32x32_bf16 v[32:47], v[60:63], v[8:15], %[ix]\n" \
  "s_mov_b64 exec, %[vm]\n" \
  "ds_read_b128 v[60:63], %[va] offset:17920\n" \
  "s_mov_b64 exec, -1\n" \
  "v_exp_f32 v18, v18\n" \
  "v_exp_f32 v19, v19\n" \
  "buffer_load_dwordx4 v[116:119], %[ka], %[sw], %[w1t] offen offset:1024 sc1\n" \
  "v_exp_f32 v20, v20\n" \
  "v_exp_f32 v21, v21\n" \
  "s_waitcnt lgkmcnt(2)\n" \
  "v_mfma_f32_32x32x16_bf16 v[0:15], v[52:55], v[48:51], 0\n" \
  "ds_read_b128 v[52:55], %[ka] offset:19456\n" \
  "v_exp_f32 v22, v22\n" \
  "v_exp_f32 v23, v23\n" \
  "v_exp_f32 v24, v24\n" \
  "v_exp_f32 v25, v25\n" \
  "s_waitcnt lgkmcnt(2)\n" \
  "v_smfmac_f32_32x32x32_bf16 v[32:47], v[56:59], v[16:23], %[ix]\n" \
  "s_mov_b64 exec, %[vm]\n" \
  "ds_read_b128 v[56:59], %[va] offset:18432\n" \
  "s_mov_b64 exec, -1\n" \
  "ds_read_b32 %[ct], %[ca]\n" \
  "v_exp_f32 v26, v26\n" \
  "v_exp_f32 v27, v27\n" \
  "v_exp_f32 v28, v28\n" \
  "v_exp_f32 v29, v29\n" \
  "v_exp_f32 v30, v30\n" \
  "v_exp_f32 v31, v31\n" \
  "s_setprio 1\n" \
  "v_exp_f32 v0, v0\n" \
  "v_exp_f32 v1, v1\n" \
  "s_waitcnt lgkmcnt(3)\n" \
  "v_smfmac_f32_32x32x32_bf16 v[32:47], v[60:63], v[24:31], %[ix]\n" \
  "s_mov_b64 exec, %[vm]\n" \
  "ds_read_b128 v[60:63], %[va] offset:18944\n" \
  "s_mov_b64 exec, -1\n" \
  "v_exp_f32 v2, v2\n" \
  "v_exp_f32 v3, v3\n" \
  "s_waitcnt lgkmcnt(1)\n" \
  "v_readfirstlane_b32 %[t1], %[ct]\n" \
  "s_cmp_ge_u32 %[t1], 1135940\n" \
  "s_cbranch_scc0 Lspin5_%=\n" \
  "Lback5_%=:\n" \
  "buffer_load_dwordx4 v[120:123], %[ka], %[sw], %[w1t] offen offset:2048 sc1\n" \
  "v_exp_f32 v4, v4\n" \
  "v_exp_f32 v5, v5\n" \
  "v_mfma_f32_32x32x16_bf16 v[16:31], v[52:55], v[48:51], 0\n" \
  "ds_read_b128 v[52:55], %[ka] offset:32768\n" \
  "v_exp_f32 v6, v6\n" \
  "v_exp_f32 v7, v7\n" \
  "v_exp_f32 v8, v8\n" \
  "v_exp_f32 v9, v9\n" \
  "v_smfmac_f32_32x32x32_bf16 v[32:47], v[56:59], v[0:7], %[ix]\n" \
  "s_mov_b64 exec, %[vm]\n" \
  "ds_read_b128 v[56:59], %[va] offset:19456\n" \
  "s_mov_b64 exec, -1\n" \
  "v_exp_f32 v10, v10\n" \
  "v_exp_f32 v11, v11\n" \
  "v_exp_f32 v12, v12\n" \
  "s_add_u32 %[rot], %[rot], 8192\n" \
  "s_and_b32 %[rot], %[rot], 0x1e000\n" \
  "s_add_u32 m0, %[kvd], 0\n" \
  "s_add_u32 %[t0], %[rot], %[kvs]\n" \
  "buffer_load_dwordx4 %[ka], %[sw], %[t0] offen sc1 lds\n" \
  "v_exp_f32 v13, v13\n" \
  "v_exp_f32 v14, v14\n" \
  "v_exp_f32 v15, v15\n" \
  "s_setprio 0\n" \
  "v_exp_f32 v16, v16\n" \
  "v_exp_f32 v17, v17\n" \
  "s_waitcnt lgkmcnt(2)\n" \
  "v_smfmac_f32_32x32x32_bf16 v[32:47], v[60:63], v[8:15], %[ix]\n" \
  "s_mov_b64 exec, %[vm]\n" \
  "ds_read_b128 v[60:63], %[va] offset:19968\n" \
  "s_mov_b64 exec, -1\n" \
  "v_exp_f32 v18, v18\n" \
  "v_exp_f32 v19, v19\n" \
  "buffer_load_dwordx4 v[124:127], %[ka], %[sw], %[w1t] offen offset:3072 sc1\n" \
  "v_exp_f32 v20, v20\n" \
  "v_exp_f32 v21, v21\n" \
  "s_waitcnt lgkmcnt(2)\n" \
  "v_mfma_f32_32x32x16_bf16 v[0:15], v[52:55], v[48:51], 0\n" \
  "ds_read_b128 v[52:55], %[ka] offset:33792\n" \
  "v_exp_f32 v22, v22\n" \
  "v_exp_f32 v23, v23\n" \
  "v_exp_f32 v24, v24\n" \
  "v_exp_f32 v25, v25\n" \
  "s_waitcnt lgkmcnt(2)\n" \
  "v_smfmac_f32_32x32x32_bf16 v[32:47], v[56:59], v[16:23], %[ix]\n" \
  "s_mov_b64 exec, %[vm]\n" \
  "ds_read_b128 v[56:59], %[va] offset:32768\n" \
  "s_mov_b64 exec, -1\n" \
  "v_exp_f32 v26, v26\n" \
  "v_exp_f32 v27, v27\n" \
  "v_exp_f32 v28, v28\n" \
  "s_add_u32 m0, %[kvd], 0\n" \
  "s_add_u32 %[t0], %[rot], %[kvs]\n" \
  "buffer_load_dwordx4 %[ka], %[sw], %[t0] offen offset:1024 sc1 lds\n" \
  "v_exp_f32 v29, v29\n" \
  "v_exp_f32 v30, v30\n" \
  "v_exp_f32 v31, v31\n" \
  "s_setprio 3\n" \
  "v_exp_f32 v0, v0\n" \
  "v_exp_f32 v1, v1\n" \
  "s_waitcnt lgkmcnt(2)\n" \
  "v_smfmac_f32_32x32x32_bf16 v[32:47], v[60:63], v[24:31], %[ix]\n" \
  "s_mov_b64 exec, %[vm]\n" \
  "ds_read_b128 v[60:63], %[va] offset:33280\n" \
  "s_mov_b64 exec, 1\n" \
  "s_waitcnt vmcnt(6)\n" \
  "ds_add_u32 %[ca], %[ix]\n" \
  "s_mov_b64 exec, -1\n" \
  "v_exp_f32 v2, v2\n" \
  "v_exp_f32 v3, v3\n" \
  "s_add_u32 %[w1t], %[w1t], 4096\n" \
  "buffer_load_dwordx4 v[128:131], %[ka], %[sw], %[w1t] offen offset:0 sc1\n" \
  "v_exp_f32 v4, v4\n" \
  "v_exp_f32 v5, v5\n" \
  "s_waitcnt lgkmcnt(3)\n" \
  "v_mfma_f32_32x32x16_bf16 v[16:31], v[52:55], v[48:51], 0\n" \
  "ds_read_b128 v[52:55], %[ka] offset:34816\n" \
  "v_exp_f32 v6, v6\n" \
  "v_exp_f32 v7, v7\n" \
  "v_exp_f32 v8, v8\n" \
  "v_exp_f32 v9, v9\n" \
  "s_waitcnt lgkmcnt(3)\n" \
  "v_smfmac_f32_32x32x32_bf16 v[32:47], v[56:59], v[0:7], %[ix]\n" \
  "s_mov_b64 exec, %[vm]\n" \
  "ds_read_b128 v[56:59], %[va] offset:33792\n" \
  "s_mov_b64 exec, -1\n" \
  "v_exp_f32 v10, v10\n" \
  "v_exp_f32 v11, v11\n" \
  "v_exp_f32 v12, v12\n" \
  "v_exp_f32 v13, v13\n" \
  "v_exp_f32 v14, v14\n" \
  "v_exp_f32 v15, v15\n" \
  "s_setprio 2\n" \
  "v_exp_f32 v16, v16\n" \
  "v_exp_f32 v17, v17\n" \
  "s_waitcnt lgkmcnt(3)\n" \
  "v_smfmac_f32_32x32x32_bf16 v[32:47], v[60:63], v[8:15], %[ix]\n" \
  "s_mov_b64 exec, %[vm]\n" \
  "ds_read_b128 v[60:63], %[va] offset:34304\n" \
  "s_mov_b64 exec, -1\n" \
  "v_exp_f32 v18, v18\n" \
  "v_exp_f32 v19, v19\n" \
  "buffer_load_dwordx4 v[132:135], %[ka], %[sw], %[w1t] offen offset:1024 sc1\n" \
  "v_exp_f32 v20, v20\n" \
  "v_exp_f32 v21, v21\n" \
  "s_waitcnt lgkmcnt(2)\n" \
  "v_mfma_f32_32x32x16_bf16 v[0:15], v[52:55], v[48:51], 0\n" \
  "ds_read_b128 v[52:55], %[ka] offset:35840\n" \
  "v_exp_f32 v22, v22\n" \
  "v_exp_f32 v23, v23\n" \
  "v_exp_f32 v24, v24\n" \
  "v_exp_f32 v25, v25\n" \
  "s_waitcnt lgkmcnt(2)\n" \
  "v_smfmac_f32_32x32x32_bf16 v[32:47], v[56:59], v[16:23], %[ix]\n" \
  "s_mov_b64 exec, %[vm]\n" \
  "ds_read_b128 v[56:59], %[va] offset:34816\n" \
  "s_mov_b64 exec, -1\n" \
  "ds_read_b32 %[ct], %[ca]\n" \
  "v_exp_f32 v26, v26\n" \
  "v_exp_f32 v27, v27\n" \
  "v_exp_f32 v28, v28\n" \
  "v_exp_f32 v29, v29\n" \
  "v_exp_f32 v30, v30\n" \
  "v_exp_f32 v31, v31\n" \
  "s_setprio 1\n" \
  "v_exp_f32 v0, v0\n" \
  "v_exp_f32 v1, v1\n" \
  "s_waitcnt lgkmcnt(3)\n" \
  "v_smfmac_f32_32x32x32_bf16 v[32:47], v[60:63], v[24:31], %[ix]\n" \
  "s_mov_b64 exec, %[vm]\n" \
  "ds_read_b128 v[60:63], %[va] offset:35328\n" \
  "s_mov_b64 exec, -1\n" \
  "v_exp_f32 v2, v2\n" \
  "v_exp_f32 v3, v3\n" \
  "s_waitcnt lgkmcnt(1)\n" \
  "v_readfirstlane_b32 %[t1], %[ct]\n" \
  "s_cmp_ge_u32 %[t1], 1363128\n" \
  "s_cbranch_scc0 Lspin6_%=\n" \
  "Lback6_%=:\n" \
  "buffer_load_dwordx4 v[136:139], %[ka], %[sw], %[w1t] offen offset:2048 sc1\n" \
  "v_exp_f32 v4, v4\n" \
  "v_exp_f32 v5, v5\n" \
  "v_mfma_f32_32x32x16_bf16 v[16:31], v[52:55], v[48:51], 0\n" \
  "ds_read_b128 v[52:55], %[ka] offset:49152\n" \
  "v_exp_f32 v6, v6\n" \
  "v_exp_f32 v7, v7\n" \
  "v_exp_f32 v8, v8\n" \
  "v_exp_f32 v9, v9\n" \
  "v_smfmac_f32_32x32x32_bf16 v[32:47], v[56:59], v[0:7], %[ix]\n" \
  "s_mov_b64 exec, %[vm]\n" \
  "ds_read_b128 v[56:59], %[va] offset:35840\n" \
  "s_mov_b64 exec, -1\n" \
  "v_exp_f32 v10, v10\n" \
  "v_exp_f32 v11, v11\n" \
  "v_exp_f32 v12, v12\n" \
  "s_add_u32 %[rot], %[rot], 8192\n" \
  "s_and_b32 %[rot], %[rot], 0x1e000\n" \
  "s_add_u32 m0, %[kvd], 16384\n" \
  "s_add_u32 %[t0], %[rot], %[kvs]\n" \
  "buffer_load_dwordx4 %[ka], %[sw], %[t0] offen sc1 lds\n" \
  "v_exp_f32 v13, v13\n" \
  "v_exp_f32 v14, v14\n" \
  "v_exp_f32 v15, v15\n" \
  "s_setprio 0\n" \
  "v_exp_f32 v16, v16\n" \
  "v_exp_f32 v17, v17\n" \
  "s_waitcnt lgkmcnt(2)\n" \
  "v_smfmac_f32_32x32x32_bf16 v[32:47], v[60:63], v[8:15], %[ix]\n" \
  "s_mov_b64 exec, %[vm]\n" \
  "ds_read_b128 v[60:63], %[va] offset:36352\n" \
  "s_mov_b64 exec, -1\n" \
  "v_exp_f32 v18, v18\n" \
  "v_exp_f32 v19, v19\n" \
  "buffer_load_dwordx4 v[140:143], %[ka], %[sw], %[w1t] offen offset:3072 sc1\n" \
  "v_exp_f32 v20, v20\n" \
  "v_exp_f32 v21, v21\n" \
  "s_waitcnt lgkmcnt(2)\n" \
  "v_mfma_f32_32x32x16_bf16 v[0:15], v[52:55], v[48:51], 0\n" \
  "ds_read_b128 v[52:55], %[ka] offset:50176\n" \
  "v_exp_f32 v22, v22\n" \
  "v_exp_f32 v23, v23\n" \
  "v_exp_f32 v24, v24\n" \
  "v_exp_f32 v25, v25\n" \
  "s_waitcnt lgkmcnt(2)\n" \
  "v_smfmac_f32_32x32x32_bf16 v[32:47], v[56:59], v[16:23], %[ix]\n" \
  "s_mov_b64 exec, %[vm]\n" \
  "ds_read_b128 v[56:59], %[va] offset:49152\n" \
  "s_mov_b64 exec, -1\n" \
  "v_exp_f32 v26, v26\n" \
  "v_exp_f32 v27, v27\n" \
  "v_exp_f32 v28, v28\n" \
  "s_add_u32 m0, %[kvd], 16384\n" \
  "s_add_u32 %[t0], %[rot], %[kvs]\n" \
  "buffer_load_dwordx4 %[ka], %[sw], %[t0] offen offset:1024 sc1 lds\n" \
  "v_exp_f32 v29, v29\n" \
  "v_exp_f32 v30, v30\n" \
  "v_exp_f32 v31, v31\n" \
  "s_setprio 3\n" \
  "v_exp_f32 v0, v0\n" \
  "v_exp_f32 v1, v1\n" \
  "s_waitcnt lgkmcnt(2)\n" \
  "v_smfmac_f32_32x32x32_bf16 v[32:47], v[60:63], v[24:31], %[ix]\n" \
  "s_mov_b64 exec, %[vm]\n" \
  "ds_read_b128 v[60:63], %[va] offset:49664\n" \
  "s_mov_b64 exec, 1\n" \
  "s_waitcnt vmcnt(6)\n" \
  "ds_add_u32 %[ca], %[ix]\n" \
  "s_mov_b64 exec, -1\n" \
  "v_exp_f32 v2, v2\n" \
  "v_exp_f32 v3, v3\n" \
  "s_add_u32 %[w1t], %[w1t], 4096\n" \
  "buffer_load_dwordx4 v[144:147], %[ka], %[sw], %[w1t] offen offset:0 sc1\n" \
  "v_exp_f32 v4, v4\n" \
  "v_exp_f32 v5, v5\n" \
  "s_waitcnt lgkmcnt(3)\n" \
  "v_mfma_f32_32x32x16_bf16 v[16:31], v[52:55], v[48:51], 0\n" \
  "ds_read_b128 v[52:55], %[ka] offset:51200\n" \
  "v_exp_f32 v6, v6\n" \
  "v_exp_f32 v7, v7\n" \
  "v_exp_f32 v8, v8\n" \
  "v_exp_f32 v9, v9\n" \
  "s_waitcnt lgkmcnt(3)\n" \
  "v_smfmac_f32_32x32x32_bf16 v[32:47], v[56:59], v[0:7], %[ix]\n" \
  "s_mov_b64 exec, %[vm]\n" \
  "ds_read_b128 v[56:59], %[va] offset:50176\n" \
  "s_mov_b64 exec, -1\n" \
  "v_exp_f32 v10, v10\n" \
  "v_exp_f32 v11, v11\n" \
  "v_exp_f32 v12, v12\n" \
  "v_exp_f32 v13, v13\n" \
  "v_exp_f32 v14, v14\n" \
  "v_exp_f32 v15, v15\n" \
  "s_setprio 2\n" \
  "v_exp_f32 v16, v16\n" \
  "v_exp_f32 v17, v17\n" \
  "s_waitcnt lgkmcnt(3)\n" \
  "v_smfmac_f32_32x32x32_bf16 v[32:47], v[60:63], v[8:15], %[ix]\n" \
  "s_mov_b64 exec, %[vm]\n" \
  "ds_read_b128 v[60:63], %[va] offset:50688\n" \
  "s_mov_b64 exec, -1\n" \
  "v_exp_f32 v18, v18\n" \
  "v_exp_f32 v19, v19\n" \
  "buffer_load_dwordx4 v[148:151], %[ka], %[sw], %[w1t] offen offset:1024 sc1\n" \
  "v_exp_f32 v20, v20\n" \
  "v_exp_f32 v21, v21\n" \
  "s_waitcnt lgkmcnt(2)\n" \
  "v_mfma_f32_32x32x16_bf16 v[0:15], v[52:55], v[48:51], 0\n" \
  "ds_read_b128 v[52:55], %[ka] offset:52224\n" \
  "v_exp_f32 v22, v22\n" \
  "v_exp_f32 v23, v23\n" \
  "v_exp_f32 v24, v24\n" \
  "v_exp_f32 v25, v25\n" \
  "s_waitcnt lgkmcnt(2)\n" \
  "v_smfmac_f32_32x32x32_bf16 v[32:47], v[56:59], v[16:23], %[ix]\n" \
  "s_mov_b64 exec, %[vm]\n" \
  "ds_read_b128 v[56:59], %[va] offset:51200\n" \
  "s_mov_b64 exec, -1\n" \
  "ds_read_b32 %[ct], %[ca]\n" \
  "v_exp_f32 v26, v26\n" \
  "v_exp_f32 v27, v27\n" \
  "v_exp_f32 v28, v28\n" \
  "v_exp_f32 v29, v29\n" \
  "v_exp_f32 v30, v30\n" \
  "v_exp_f32 v31, v31\n" \
  "s_setprio 1\n" \
  "v_exp_f32 v0, v0\n" \
  "v_exp_f32 v1, v1\n" \
  "s_waitcnt lgkmcnt(3)\n" \
  "v_smfmac_f32_32x32x32_bf16 v[32:47], v[60:63], v[24:31], %[ix]\n" \
  "s_mov_b64 exec, %[vm]\n" \
  "ds_read_b128 v[60:63], %[va] offset:51712\n" \
  "s_mov_b64 exec, -1\n" \
  "v_exp_f32 v2, v2\n" \
  "v_exp_f32 v3, v3\n" \
  "s_waitcnt lgkmcnt(1)\n" \
  "v_readfirstlane_b32 %[t1], %[ct]\n" \
  "s_cmp_ge_u32 %[t1], 1590316\n" \
  "s_cbranch_scc0 Lspin7_%=\n" \
  "Lback7_%=:\n" \
  "buffer_load_dwordx4 v[152:155], %[ka], %[sw], %[w1t] offen offset:2048 sc1\n" \
  "v_exp_f32 v4, v4\n" \
  "v_exp_f32 v5, v5\n" \
  "v_mfma_f32_32x32x16_bf16 v[16:31], v[52:55], v[48:51], 0\n" \
  "ds_read_b128 v[52:55], %[ka] offset:0\n" \
  "v_exp_f32 v6, v6\n" \
  "v_exp_f32 v7, v7\n" \
  "v_exp_f32 v8, v8\n" \
  "v_exp_f32 v9, v9\n" \
  "v_smfmac_f32_32x32x32_bf16 v[32:47], v[56:59], v[0:7], %[ix]\n" \
  "s_mov_b64 exec, %[vm]\n" \
  "ds_read_b128 v[56:59], %[va] offset:52224\n" \
  "s_mov_b64 exec, -1\n" \
  "v_exp_f32 v10, v10\n" \
  "v_exp_f32 v11, v11\n" \
  "v_exp_f32 v12, v12\n" \
  "s_add_u32 %[rot], %[rot], 8192\n" \
  "s_and_b32 %[rot], %[rot], 0x1e000\n" \
  "s_add_u32 m0, %[kvd], 32768\n" \
  "s_add_u32 %[t0], %[rot], %[kvs]\n" \
  "buffer_load_dwordx4 %[ka], %[sw], %[t0] offen sc1 lds\n" \
  "v_exp_f32 v13, v13\n" \
  "v_exp_f32 v14, v14\n" \
  "v_exp_f32 v15, v15\n" \
  "s_setprio 0\n" \
  "v_exp_f32 v16, v16\n" \
  "v_exp_f32 v17, v17\n" \
  "s_waitcnt lgkmcnt(2)\n" \
  "v_smfmac_f32_32x32x32_bf16 v[32:47], v[60:63], v[8:15], %[ix]\n" \
  "s_mov_b64 exec, %[vm]\n" \
  "ds_read_b128 v[60:63], %[va] offset:52736\n" \
  "s_mov_b64 exec, -1\n" \
  "v_exp_f32 v18, v18\n" \
  "v_exp_f32 v19, v19\n" \
  "buffer_load_dwordx4 v[156:159], %[ka], %[sw], %[w1t] offen offset:3072 sc1\n" \
  "v_exp_f32 v20, v20\n" \
  "v_exp_f32 v21, v21\n" \
  "s_waitcnt lgkmcnt(2)\n" \
  "v_mfma_f32_32x32x16_bf16 v[0:15], v[52:55], v[48:51], 0\n" \
  "ds_read_b128 v[52:55], %[ka] offset:1024\n" \
  "v_exp_f32 v22, v22\n" \
  "v_exp_f32 v23, v23\n" \
  "v_exp_f32 v24, v24\n" \
  "v_exp_f32 v25, v25\n" \
  "s_waitcnt lgkmcnt(2)\n" \
  "v_smfmac_f32_32x32x32_bf16 v[32:47], v[56:59], v[16:23], %[ix]\n" \
  "s_mov_b64 exec, %[vm]\n" \
  "ds_read_b128 v[56:59], %[va] offset:0\n" \
  "s_mov_b64 exec, -1\n" \
  "v_exp_f32 v26, v26\n" \
  "v_exp_f32 v27, v27\n" \
  "v_exp_f32 v28, v28\n" \
  "s_add_u32 m0, %[kvd], 32768\n" \
  "s_add_u32 %[t0], %[rot], %[kvs]\n" \
  "buffer_load_dwordx4 %[ka], %[sw], %[t0] offen offset:1024 sc1 lds\n" \
  "v_exp_f32 v29, v29\n" \
  "v_exp_f32 v30, v30\n" \
  "v_exp_f32 v31, v31\n" \
  "s_setprio 3\n" \
  "v_exp_f32 v0, v0\n" \
  "v_exp_f32 v1, v1\n" \
  "s_waitcnt lgkmcnt(2)\n" \
  "v_smfmac_f32_32x32x32_bf16 v[32:47], v[60:63], v[24:31], %[ix]\n" \
  "s_mov_b64 exec, %[vm]\n" \
  "ds_read_b128 v[60:63], %[va] offset:512\n" \
  "s_mov_b64 exec, 1\n" \
  "s_waitcnt vmcnt(6)\n" \
  "ds_add_u32 %[ca], %[ix]\n" \
  "s_mov_b64 exec, -1\n" \
  "v_exp_f32 v2, v2\n" \
  "v_exp_f32 v3, v3\n" \
  "s_add_u32 %[w1t], %[w1t], 4096\n" \
  "buffer_load_dwordx4 v[160:163], %[ka], %[sw], %[w1t] offen offset:0 sc1\n" \
  "v_exp_f32 v4, v4\n" \
  "v_exp_f32 v5, v5\n" \
  "s_waitcnt lgkmcnt(3)\n" \
  "v_mfma_f32_32x32x16_bf16 v[16:31], v[52:55], v[48:51], 0\n" \
  "ds_read_b128 v[52:55], %[ka] offset:2048\n" \
  "v_exp_f32 v6, v6\n" \
  "v_exp_f32 v7, v7\n" \
  "v_exp_f32 v8, v8\n" \
  "v_exp_f32 v9, v9\n" \
  "s_waitcnt lgkmcnt(3)\n" \
  "v_smfmac_f32_32x32x32_bf16 v[32:47], v[56:59], v[0:7], %[ix]\n" \
  "s_mov_b64 exec, %[vm]\n" \
  "ds_read_b128 v[56:59], %[va] offset:1024\n" \
  "s_mov_b64 exec, -1\n" \
  "v_exp_f32 v10, v10\n" \
  "v_exp_f32 v11, v11\n" \
  "v_exp_f32 v12, v12\n" \
  "v_exp_f32 v13, v13\n" \
  "v_exp_f32 v14, v14\n" \
  "v_exp_f32 v15, v15\n" \
  "s_setprio 2\n" \
  "v_exp_f32 v16, v16\n" \
  "v_exp_f32 v17, v17\n" \
  "s_waitcnt lgkmcnt(3)\n" \
  "v_smfmac_f32_32x32x32_bf16 v[32:47], v[60:63], v[8:15], %[ix]\n" \
  "s_mov_b64 exec, %[vm]\n" \
  "ds_read_b128 v[60:63], %[va] offset:1536\n" \
  "s_mov_b64 exec, -1\n" \
  "v_exp_f32 v18, v18\n" \
  "v_exp_f32 v19, v19\n" \
  "buffer_load_dwordx4 v[164:167], %[ka], %[sw], %[w1t] offen offset:1024 sc1\n" \
  "v_exp_f32 v20, v20\n" \
  "v_exp_f32 v21, v21\n" \
  "s_waitcnt lgkmcnt(2)\n" \
  "v_mfma_f32_32x32x16_bf16 v[0:15], v[52:55], v[48:51], 0\n" \
  "ds_read_b128 v[52:55], %[ka] offset:3072\n" \
  "v_exp_f32 v22, v22\n" \
  "v_exp_f32 v23, v23\n" \
  "v_exp_f32 v24, v24\n" \
  "v_exp_f32 v25, v25\n" \
  "s_waitcnt lgkmcnt(2)\n" \
  "v_smfmac_f32_32x32x32_bf16 v[32:47], v[56:59], v[16:23], %[ix]\n" \
  "s_mov_b64 exec, %[vm]\n" \
  "ds_read_b128 v[56:59], %[va] offset:2048\n" \
  "s_mov_b64 exec, -1\n" \
  "ds_read_b32 %[ct], %[ca]\n" \
  "v_exp_f32 v26, v26\n" \
  "v_exp_f32 v27, v27\n" \
  "v_exp_f32 v28, v28\n" \
  "v_exp_f32 v29, v29\n" \
  "v_exp_f32 v30, v30\n" \
  "v_exp_f32 v31, v31\n" \
  "s_setprio 1\n" \
  "v_exp_f32 v0, v0\n" \
  "v_exp_f32 v1, v1\n" \
  "s_waitcnt lgkmcnt(3)\n" \
  "v_smfmac_f32_32x32x32_bf16 v[32:47], v[60:63], v[24:31], %[ix]\n" \
  "s_mov_b64 exec, %[vm]\n" \
  "ds_read_b128 v[60:63], %[va] offset:2560\n" \
  "s_mov_b64 exec, -1\n" \
  "v_exp_f32 v2, v2\n" \
  "v_exp_f32 v3, v3\n" \
  "s_waitcnt lgkmcnt(1)\n" \
  "v_readfirstlane_b32 %[t1], %[ct]\n" \
  "s_cmp_ge_u32 %[t1], 1817504\n" \
  "s_cbranch_scc0 Lspin8_%=\n" \
  "Lback8_%=:\n" \
  "buffer_load_dwordx4 v[168:171], %[ka], %[sw], %[w1t] offen offset:2048 sc1\n" \
  "v_exp_f32 v4, v4\n" \
  "v_exp_f32 v5, v5\n" \
  "v_mfma_f32_32x32x16_bf16 v[16:31], v[52:55], v[48:51], 0\n" \
  "ds_read_b128 v[52:55], %[ka] offset:16384\n" \
  "v_exp_f32 v6, v6\n" \
  "v_exp_f32 v7, v7\n" \
  "v_exp_f32 v8, v8\n" \
  "v_exp_f32 v9, v9\n" \
  "v_smfmac_f32_32x32x32_bf16 v[32:47], v[56:59], v[0:7], %[ix]\n" \
  "s_mov_b64 exec, %[vm]\n" \
  "ds_read_b128 v[56:59], %[va] offset:3072\n" \
  "s_mov_b64 exec, -1\n" \
  "v_exp_f32 v10, v10\n" \
  "v_exp_f32 v11, v11\n" \
  "v_exp_f32 v12, v12\n" \
  "s_add_u32 %[rot], %[rot], 8192\n" \
  "s_and_b32 %[rot], %[rot], 0x1e000\n" \
  "s_add_u32 m0, %[kvd], 49152\n" \
  "s_add_u32 %[t0], %[rot], %[kvs]\n" \
  "buffer_load_dwordx4 %[ka], %[sw], %[t0] offen sc1 lds\n" \
  "v_exp_f32 v13, v13\n" \
  "v_exp_f32 v14, v14\n" \
  "v_exp_f32 v15, v15\n" \
  "s_setprio 0\n" \
  "v_exp_f32 v16, v16\n" \
  "v_exp_f32 v17, v17\n" \
  "s_waitcnt lgkmcnt(2)\n" \
  "v_smfmac_f32_32x32x32_bf16 v[32:47], v[60:63], v[8:15], %[ix]\n" \
  "s_mov_b64 exec, %[vm]\n" \
  "ds_read_b128 v[60:63], %[va] offset:3584\n" \
  "s_mov_b64 exec, -1\n" \
  "v_exp_f32 v18, v18\n" \
  "v_exp_f32 v19, v19\n" \
  "buffer_load_dwordx4 v[172:175], %[ka], %[sw], %[w1t] offen offset:3072 sc1\n" \
  "v_exp_f32 v20, v20\n" \
  "v_exp_f32 v21, v21\n" \
  "s_waitcnt lgkmcnt(2)\n" \
  "v_mfma_f32_32x32x16_bf16 v[0:15], v[52:55], v[48:51], 0\n" \
  "ds_read_b128 v[52:55], %[ka] offset:17408\n" \
  "v_exp_f32 v22, v22\n" \
  "v_exp_f32 v23, v23\n" \
  "v_exp_f32 v24, v24\n" \
  "v_exp_f32 v25, v25\n" \
  "s_waitcnt lgkmcnt(2)\n" \
  "v_smfmac_f32_32x32x32_bf16 v[32:47], v[56:59], v[16:23], %[ix]\n" \
  "s_mov_b64 exec, %[vm]\n" \
  "ds_read_b128 v[56:59], %[va] offset:16384\n" \
  "s_mov_b64 exec, -1\n" \
  "v_exp_f32 v26, v26\n" \
  "v_exp_f32 v27, v27\n" \
  "v_exp_f32 v28, v28\n" \
  "s_add_u32 m0, %[kvd], 49152\n" \
  "s_add_u32 %[t0], %[rot], %[kvs]\n" \
  "buffer_load_dwordx4 %[ka], %[sw], %[t0] offen offset:1024 sc1 lds\n" \
  "v_exp_f32 v29, v29\n" \
  "v_exp_f32 v30, v30\n" \
  "v_exp_f32 v31, v31\n" \
  "s_setprio 3\n" \
  "v_exp_f32 v0, v0\n" \
  "v_exp_f32 v1, v1\n" \
  "s_waitcnt lgkmcnt(2)\n" \
  "v_smfmac_f32_32x32x32_bf16 v[32:47], v[60:63], v[24:31], %[ix]\n" \
  "s_mov_b64 exec, %[vm]\n" \
  "ds_read_b128 v[60:63], %[va] offset:16896\n" \
  "s_mov_b64 exec, 1\n" \
  "s_waitcnt vmcnt(6)\n" \
  "ds_add_u32 %[ca], %[ix]\n" \
  "s_mov_b64 exec, -1\n" \
  "v_exp_f32 v2, v2\n" \
  "v_exp_f32 v3, v3\n" \
  "s_add_u32 %[w1t], %[w1t], 4096\n" \
  "buffer_load_dwordx4 v[176:179], %[ka], %[sw], %[w1t] offen offset:0 sc1\n" \
  "v_exp_f32 v4, v4\n" \
  "v_exp_f32 v5, v5\n" \
  "s_waitcnt lgkmcnt(3)\n" \
  "v_mfma_f32_32x32x16_bf16 v[16:31], v[52:55], v[48:51], 0\n" \
  "ds_read_b128 v[52:55], %[ka] offset:18432\n" \
  "v_exp_f32 v6, v6\n" \
  "v_exp_f32 v7, v7\n" \
  "v_exp_f32 v8, v8\n" \
  "v_exp_f32 v9, v9\n" \
  "s_waitcnt lgkmcnt(3)\n" \
  "v_smfmac_f32_32x32x32_bf16 v[32:47], v[56:59], v[0:7], %[ix]\n" \
  "s_mov_b64 exec, %[vm]\n" \
  "ds_read_b128 v[56:59], %[va] offset:17408\n" \
  "s_mov_b64 exec, -1\n" \
  "v_exp_f32 v10, v10\n" \
  "v_exp_f32 v11, v11\n" \
  "v_exp_f32 v12, v12\n" \
  "v_exp_f32 v13, v13\n" \
  "v_exp_f32 v14, v14\n" \
  "v_exp_f32 v15, v15\n" \
  "s_setprio 2\n" \
  "v_exp_f32 v16, v16\n" \
  "v_exp_f32 v17, v17\n" \
  "s_waitcnt lgkmcnt(3)\n" \
  "v_smfmac_f32_32x32x32_bf16 v[32:47], v[60:63], v[8:15], %[ix]\n" \
  "s_mov_b64 exec, %[vm]\n" \
  "ds_read_b128 v[60:63], %[va] offset:17920\n" \
  "s_mov_b64 exec, -1\n" \
  "v_exp_f32 v18, v18\n" \
  "v_exp_f32 v19, v19\n" \
  "buffer_load_dwordx4 v[180:183], %[ka], %[sw], %[w1t] offen offset:1024 sc1\n" \
  "v_exp_f32 v20, v20\n" \
  "v_exp_f32 v21, v21\n" \
  "s_waitcnt lgkmcnt(2)\n" \
  "v_mfma_f32_32x32x16_bf16 v[0:15], v[52:55], v[48:51], 0\n" \
  "ds_read_b128 v[52:55], %[ka] offset:19456\n" \
  "v_exp_f32 v22, v22\n" \
  "v_exp_f32 v23, v23\n" \
  "v_exp_f32 v24, v24\n" \
  "v_exp_f32 v25, v25\n" \
  "s_waitcnt lgkmcnt(2)\n" \
  "v_smfmac_f32_32x32x32_bf16 v[32:47], v[56:59], v[16:23], %[ix]\n" \
  "s_mov_b64 exec, %[vm]\n" \
  "ds_read_b128 v[56:59], %[va] offset:18432\n" \
  "s_mov_b64 exec, -1\n" \
  "ds_read_b32 %[ct], %[ca]\n" \
  "v_exp_f32 v26, v26\n" \
  "v_exp_f32 v27, v27\n" \
  "v_exp_f32 v28, v28\n" \
  "v_exp_f32 v29, v29\n" \
  "v_exp_f32 v30, v30\n" \
  "v_exp_f32 v31, v31\n" \
  "s_setprio 1\n" \
  "v_exp_f32 v0, v0\n" \
  "v_exp_f32 v1, v1\n" \
  "s_waitcnt lgkmcnt(3)\n" \
  "v_smfmac_f32_32x32x32_bf16 v[32:47], v[60:63], v[24:31], %[ix]\n" \
  "s_mov_b64 exec, %[vm]\n" \
  "ds_read_b128 v[60:63], %[va] offset:18944\n" \
  "s_mov_b64 exec, -1\n" \
  "v_exp_f32 v2, v2\n" \
  "v_exp_f32 v3, v3\n" \
  "s_waitcnt lgkmcnt(1)\n" \
  "v_readfirstlane_b32 %[t1], %[ct]\n" \
  "s_cmp_ge_u32 %[t1], 2044692\n" \
  "s_cbranch_scc0 Lspin9_%=\n" \
  "Lback9_%=:\n" \
  "buffer_load_dwordx4 v[184:187], %[ka], %[sw], %[w1t] offen offset:2048 sc1\n" \
  "v_exp_f32 v4, v4\n" \
  "v_exp_f32 v5, v5\n" \
  "v_mfma_f32_32x32x16_bf16 v[16:31], v[52:55], v[48:51], 0\n" \
  "ds_read_b128 v[52:55], %[ka] offset:32768\n" \
  "v_exp_f32 v6, v6\n" \
  "v_exp_f32 v7, v7\n" \
  "v_exp_f32 v8, v8\n" \
  "v_exp_f32 v9, v9\n" \
  "v_smfmac_f32_32x32x32_bf16 v[32:47], v[56:59], v[0:7], %[ix]\n" \
  "s_mov_b64 exec, %[vm]\n" \
  "ds_read_b128 v[56:59], %[va] offset:19456\n" \
  "s_mov_b64 exec, -1\n" \
  "v_exp_f32 v10, v10\n" \
  "v_exp_f32 v11, v11\n" \
  "v_exp_f32 v12, v12\n" \
  "s_add_u32 %[rot], %[rot], 8192\n" \
  "s_and_b32 %[rot], %[rot], 0x1e000\n" \
  "s_add_u32 m0, %[kvd], 0\n" \
  "s_add_u32 %[t0], %[rot], %[kvs]\n" \
  "buffer_load_dwordx4 %[ka], %[sw], %[t0] offen sc1 lds\n" \
  "v_exp_f32 v13, v13\n" \
  "v_exp_f32 v14, v14\n" \
  "v_exp_f32 v15, v15\n" \
  "s_setprio 0\n" \
  "v_exp_f32 v16, v16\n" \
  "v_exp_f32 v17, v17\n" \
  "s_waitcnt lgkmcnt(2)\n" \
  "v_smfmac_f32_32x32x32_bf16 v[32:47], v[60:63], v[8:15], %[ix]\n" \
  "s_mov_b64 exec, %[vm]\n" \
  "ds_read_b128 v[60:63], %[va] offset:19968\n" \
  "s_mov_b64 exec, -1\n" \
  "v_exp_f32 v18, v18\n" \
  "v_exp_f32 v19, v19\n" \
  "buffer_load_dwordx4 v[188:191], %[ka], %[sw], %[w1t] offen offset:3072 sc1\n" \
  "v_exp_f32 v20, v20\n" \
  "v_exp_f32 v21, v21\n" \
  "s_waitcnt lgkmcnt(2)\n" \
  "v_mfma_f32_32x32x16_bf16 v[0:15], v[52:55], v[48:51], 0\n" \
  "ds_read_b128 v[52:55], %[ka] offset:33792\n" \
  "v_exp_f32 v22, v22\n" \
  "v_exp_f32 v23, v23\n" \
  "v_exp_f32 v24, v24\n" \
  "v_exp_f32 v25, v25\n" \
  "s_waitcnt lgkmcnt(2)\n" \
  "v_smfmac_f32_32x32x32_bf16 v[32:47], v[56:59], v[16:23], %[ix]\n" \
  "s_mov_b64 exec, %[vm]\n" \
  "ds_read_b128 v[56:59], %[va] offset:32768\n" \
  "s_mov_b64 exec, -1\n" \
  "v_exp_f32 v26, v26\n" \
  "v_exp_f32 v27, v27\n" \
  "v_exp_f32 v28, v28\n" \
  "s_add_u32 m0, %[kvd], 0\n" \
  "s_add_u32 %[t0], %[rot], %[kvs]\n" \
  "buffer_load_dwordx4 %[ka], %[sw], %[t0] offen offset:1024 sc1 lds\n" \
  "v_exp_f32 v29, v29\n" \
  "v_exp_f32 v30, v30\n" \
  "v_exp_f32 v31, v31\n" \
  "s_setprio 3\n" \
  "v_exp_f32 v0, v0\n" \
  "v_exp_f32 v1, v1\n" \
  "s_waitcnt lgkmcnt(2)\n" \
  "v_smfmac_f32_32x32x32_bf16 v[32:47], v[60:63], v[24:31], %[ix]\n" \
  "s_mov_b64 exec, %[vm]\n" \
  "ds_read_b128 v[60:63], %[va] offset:33280\n" \
  "s_mov_b64 exec, 1\n" \
  "s_waitcnt vmcnt(6)\n" \
  "ds_add_u32 %[ca], %[ix]\n" \
  "s_mov_b64 exec, -1\n" \
  "v_exp_f32 v2, v2\n" \
  "v_exp_f32 v3, v3\n" \
  "buffer_load_dwordx4 v[192:195], %[ka], %[sw], %[w2t] offen offset:0 sc1\n" \
  "v_exp_f32 v4, v4\n" \
  "v_exp_f32 v5, v5\n" \
  "s_waitcnt lgkmcnt(3)\n" \
  "v_mfma_f32_32x32x16_bf16 v[16:31], v[52:55], v[48:51], 0\n" \
  "ds_read_b128 v[52:55], %[ka] offset:34816\n" \
  "v_exp_f32 v6, v6\n" \
  "v_exp_f32 v7, v7\n" \
  "v_exp_f32 v8, v8\n" \
  "v_exp_f32 v9, v9\n" \
  "s_waitcnt lgkmcnt(3)\n" \
  "v_smfmac_f32_32x32x32_bf16 v[32:47], v[56:59], v[0:7], %[ix]\n" \
  "s_mov_b64 exec, %[vm]\n" \
  "ds_read_b128 v[56:59], %[va] offset:33792\n" \
  "s_mov_b64 exec, -1\n" \
  "v_exp_f32 v10, v10\n" \
  "v_exp_f32 v11, v11\n" \
  "v_exp_f32 v12, v12\n" \
  "v_exp_f32 v13, v13\n" \
  "v_exp_f32 v14, v14\n" \
  "v_exp_f32 v15, v15\n" \
  "s_setprio 2\n" \
  "v_exp_f32 v16, v16\n" \
  "v_exp_f32 v17, v17\n" \
  "s_waitcnt lgkmcnt(3)\n" \
  "v_smfmac_f32_32x32x32_bf16 v[32:47], v[60:63], v[8:15], %[ix]\n" \
  "s_mov_b64 exec, %[vm]\n" \
  "ds_read_b128 v[60:63], %[va] offset:34304\n" \
  "s_mov_b64 exec, -1\n" \
  "v_exp_f32 v18, v18\n" \
  "v_exp_f32 v19, v19\n" \
  "buffer_load_dwordx4 v[196:199], %[ka], %[sw], %[w2t] offen offset:1024 sc1\n" \
  "v_exp_f32 v20, v20\n" \
  "v_exp_f32 v21, v21\n" \
  "s_waitcnt lgkmcnt(2)\n" \
  "v_mfma_f32_32x32x16_bf16 v[0:15], v[52:55], v[48:51], 0\n" \
  "ds_read_b128 v[52:55], %[ka] offset:35840\n" \
  "v_exp_f32 v22, v22\n" \
  "v_exp_f32 v23, v23\n" \
  "v_exp_f32 v24, v24\n" \
  "v_exp_f32 v25, v25\n" \
  "s_waitcnt lgkmcnt(2)\n" \
  "v_smfmac_f32_32x32x32_bf16 v[32:47], v[56:59], v[16:23], %[ix]\n" \
  "s_mov_b64 exec, %[vm]\n" \
  "ds_read_b128 v[56:59], %[va] offset:34816\n" \
  "s_mov_b64 exec, -1\n" \
  "ds_read_b32 %[ct], %[ca]\n" \
  "v_exp_f32 v26, v26\n" \
  "v_exp_f32 v27, v27\n" \
  "v_exp_f32 v28, v28\n" \
  "v_exp_f32 v29, v29\n" \
  "v_exp_f32 v30, v30\n" \
  "v_exp_f32 v31, v31\n" \
  "s_setprio 1\n" \
  "v_exp_f32 v0, v0\n" \
  "v_exp_f32 v1, v1\n" \
  "s_waitcnt lgkmcnt(3)\n" \
  "v_smfmac_f32_32x32x32_bf16 v[32:47], v[60:63], v[24:31], %[ix]\n" \
  "s_mov_b64 exec, %[vm]\n" \
  "ds_read_b128 v[60:63], %[va] offset:35328\n" \
  "s_mov_b64 exec, -1\n" \
  "v_exp_f32 v2, v2\n" \
  "v_exp_f32 v3, v3\n" \
  "s_waitcnt lgkmcnt(1)\n" \
  "v_readfirstlane_b32 %[t1], %[ct]\n" \
  "s_cmp_ge_u32 %[t1], 2271880\n" \
  "s_cbranch_scc0 Lspin10_%=\n" \
  "Lback10_%=:\n" \
  "buffer_load_dwordx4 v[200:203], %[ka], %[sw], %[w2t] offen offset:2048 sc1\n" \
  "v_exp_f32 v4, v4\n" \
  "v_exp_f32 v5, v5\n" \
  "v_mfma_f32_32x32x16_bf16 v[16:31], v[52:55], v[48:51], 0\n" \
  "ds_read_b128 v[52:55], %[ka] offset:49152\n" \
  "v_exp_f32 v6, v6\n" \
  "v_exp_f32 v7, v7\n" \
  "v_exp_f32 v8, v8\n" \
  "v_exp_f32 v9, v9\n" \
  "v_smfmac_f32_32x32x32_bf16 v[32:47], v[56:59], v[0:7], %[ix]\n" \
  "s_mov_b64 exec, %[vm]\n" \
  "ds_read_b128 v[56:59], %[va] offset:35840\n" \
  "s_mov_b64 exec, -1\n" \
  "v_exp_f32 v10, v10\n" \
  "v_exp_f32 v11, v11\n" \
  "v_exp_f32 v12, v12\n" \
  "s_add_u32 %[rot], %[rot], 8192\n" \
  "s_and_b32 %[rot], %[rot], 0x1e000\n" \
  "s_add_u32 m0, %[kvd], 16384\n" \
  "s_add_u32 %[t0], %[rot], %[kvs]\n" \
  "buffer_load_dwordx4 %[ka], %[sw], %[t0] offen sc1 lds\n" \
  "v_exp_f32 v13, v13\n" \
  "v_exp_f32 v14, v14\n" \
  "v_exp_f32 v15, v15\n" \
  "s_setprio 0\n" \
  "v_exp_f32 v16, v16\n" \
  "v_exp_f32 v17, v17\n" \
  "s_waitcnt lgkmcnt(2)\n" \
  "v_smfmac_f32_32x32x32_bf16 v[32:47], v[60:63], v[8:15], %[ix]\n" \
  "s_mov_b64 exec, %[vm]\n" \
  "ds_read_b128 v[60:63], %[va] offset:36352\n" \
  "s_mov_b64 exec, -1\n" \
  "v_exp_f32 v18, v18\n" \
  "v_exp_f32 v19, v19\n" \
  "buffer_load_dwordx4 v[204:207], %[ka], %[sw], %[w2t] offen offset:3072 sc1\n" \
  "v_exp_f32 v20, v20\n" \
  "v_exp_f32 v21, v21\n" \
  "s_waitcnt lgkmcnt(2)\n" \
  "v_mfma_f32_32x32x16_bf16 v[0:15], v[52:55], v[48:51], 0\n" \
  "ds_read_b128 v[52:55], %[ka] offset:50176\n" \
  "v_exp_f32 v22, v22\n" \
  "v_exp_f32 v23, v23\n" \
  "v_exp_f32 v24, v24\n" \
  "v_exp_f32 v25, v25\n" \
  "s_waitcnt lgkmcnt(2)\n" \
  "v_smfmac_f32_32x32x32_bf16 v[32:47], v[56:59], v[16:23], %[ix]\n" \
  "s_mov_b64 exec, %[vm]\n" \
  "ds_read_b128 v[56:59], %[va] offset:49152\n" \
  "s_mov_b64 exec, -1\n" \
  "v_exp_f32 v26, v26\n" \
  "v_exp_f32 v27, v27\n" \
  "v_exp_f32 v28, v28\n" \
  "s_add_u32 m0, %[kvd], 16384\n" \
  "s_add_u32 %[t0], %[rot], %[kvs]\n" \
  "buffer_load_dwordx4 %[ka], %[sw], %[t0] offen offset:1024 sc1 lds\n" \
  "v_exp_f32 v29, v29\n" \
  "v_exp_f32 v30, v30\n" \
  "v_exp_f32 v31, v31\n" \
  "s_setprio 3\n" \
  "v_exp_f32 v0, v0\n" \
  "v_exp_f32 v1, v1\n" \
  "s_waitcnt lgkmcnt(2)\n" \
  "v_smfmac_f32_32x32x32_bf16 v[32:47], v[60:63], v[24:31], %[ix]\n" \
  "s_mov_b64 exec, %[vm]\n" \
  "ds_read_b128 v[60:63], %[va] offset:49664\n" \
  "s_mov_b64 exec, 1\n" \
  "s_waitcnt vmcnt(6)\n" \
  "ds_add_u32 %[ca], %[ix]\n" \
  "s_mov_b64 exec, -1\n" \
  "v_exp_f32 v2, v2\n" \
  "v_exp_f32 v3, v3\n" \
  "s_add_u32 %[w2t], %[w2t], 4096\n" \
  "buffer_load_dwordx4 v[208:211], %[ka], %[sw], %[w2t] offen offset:0 sc1\n" \
  "v_exp_f32 v4, v4\n" \
  "v_exp_f32 v5, v5\n" \
  "s_waitcnt lgkmcnt(3)\n" \
  "v_mfma_f32_32x32x16_bf16 v[16:31], v[52:55], v[48:51], 0\n" \
  "ds_read_b128 v[52:55], %[ka] offset:51200\n" \
  "v_exp_f32 v6, v6\n" \
  "v_exp_f32 v7, v7\n" \
  "v_exp_f32 v8, v8\n" \
  "v_exp_f32 v9, v9\n" \
  "s_waitcnt lgkmcnt(3)\n" \
  "v_smfmac_f32_32x32x32_bf16 v[32:47], v[56:59], v[0:7], %[ix]\n" \
  "s_mov_b64 exec, %[vm]\n" \
  "ds_read_b128 v[56:59], %[va] offset:50176\n" \
  "s_mov_b64 exec, -1\n" \
  "v_exp_f32 v10, v10\n" \
  "v_exp_f32 v11, v11\n" \
  "v_exp_f32 v12, v12\n" \
  "v_exp_f32 v13, v13\n" \
  "v_exp_f32 v14, v14\n" \
  "v_exp_f32 v15, v15\n" \
  "s_setprio 2\n" \
  "v_exp_f32 v16, v16\n" \
  "v_exp_f32 v17, v17\n" \
  "s_waitcnt lgkmcnt(3)\n" \
  "v_smfmac_f32_32x32x32_bf16 v[32:47], v[60:63], v[8:15], %[ix]\n" \
  "s_mov_b64 exec, %[vm]\n" \
  "ds_read_b128 v[60:63], %[va] offset:50688\n" \
  "s_mov_b64 exec, -1\n" \
  "v_exp_f32 v18, v18\n" \
  "v_exp_f32 v19, v19\n" \
  "buffer_load_dwordx4 v[212:215], %[ka], %[sw], %[w2t] offen offset:1024 sc1\n" \
  "v_exp_f32 v20, v20\n" \
  "v_exp_f32 v21, v21\n" \
  "s_waitcnt lgkmcnt(2)\n" \
  "v_mfma_f32_32x32x16_bf16 v[0:15], v[52:55], v[48:51], 0\n" \
  "ds_read_b128 v[52:55], %[ka] offset:52224\n" \
  "v_exp_f32 v22, v22\n" \
  "v_exp_f32 v23, v23\n" \
  "v_exp_f32 v24, v24\n" \
  "v_exp_f32 v25, v25\n" \
  "s_waitcnt lgkmcnt(2)\n" \
  "v_smfmac_f32_32x32x32_bf16 v[32:47], v[56:59], v[16:23], %[ix]\n" \
  "s_mov_b64 exec, %[vm]\n" \
  "ds_read_b128 v[56:59], %[va] offset:51200\n" \
  "s_mov_b64 exec, -1\n" \
  "ds_read_b32 %[ct], %[ca]\n" \
  "v_exp_f32 v26, v26\n" \
  "v_exp_f32 v27, v27\n" \
  "v_exp_f32 v28, v28\n" \
  "v_exp_f32 v29, v29\n" \
  "v_exp_f32 v30, v30\n" \
  "v_exp_f32 v31, v31\n" \
  "s_setprio 1\n" \
  "v_exp_f32 v0, v0\n" \
  "v_exp_f32 v1, v1\n" \
  "s_waitcnt lgkmcnt(3)\n" \
  "v_smfmac_f32_32x32x32_bf16 v[32:47], v[60:63], v[24:31], %[ix]\n" \
  "s_mov_b64 exec, %[vm]\n" \
  "ds_read_b128 v[60:63], %[va] offset:51712\n" \
  "s_mov_b64 exec, -1\n" \
  "v_exp_f32 v2, v2\n" \
  "v_exp_f32 v3, v3\n" \
  "s_waitcnt lgkmcnt(1)\n" \
  "v_readfirstlane_b32 %[t1], %[ct]\n" \
  "s_cmp_ge_u32 %[t1], 2499068\n" \
  "s_cbranch_scc0 Lspin11_%=\n" \
  "Lback11_%=:\n" \
  "buffer_load_dwordx4 v[216:219], %[ka], %[sw], %[w2t] offen offset:2048 sc1\n" \
  "v_exp_f32 v4, v4\n" \
  "v_exp_f32 v5, v5\n" \
  "v_mfma_f32_32x32x16_bf16 v[16:31], v[52:55], v[48:51], 0\n" \
  "ds_read_b128 v[52:55], %[ka] offset:0\n" \
  "v_exp_f32 v6, v6\n" \
  "v_exp_f32 v7, v7\n" \
  "v_exp_f32 v8, v8\n" \
  "v_exp_f32 v9, v9\n" \
  "v_smfmac_f32_32x32x32_bf16 v[32:47], v[56:59], v[0:7], %[ix]\n" \
  "s_mov_b64 exec, %[vm]\n" \
  "ds_read_b128 v[56:59], %[va] offset:52224\n" \
  "s_mov_b64 exec, -1\n" \
  "v_exp_f32 v10, v10\n" \
  "v_exp_f32 v11, v11\n" \
  "v_exp_f32 v12, v12\n" \
  "s_add_u32 %[rot], %[rot], 8192\n" \
  "s_and_b32 %[rot], %[rot], 0x1e000\n" \
  "s_add_u32 m0, %[kvd], 32768\n" \
  "s_add_u32 %[t0], %[rot], %[kvs]\n" \
  "buffer_load_dwordx4 %[ka], %[sw], %[t0] offen sc1 lds\n" \
  "v_exp_f32 v13, v13\n" \
  "v_exp_f32 v14, v14\n" \
  "v_exp_f32 v15, v15\n" \
  "s_setprio 0\n" \
  "v_exp_f32 v16, v16\n" \
  "v_exp_f32 v17, v17\n" \
  "s_waitcnt lgkmcnt(2)\n" \
  "v_smfmac_f32_32x32x32_bf16 v[32:47], v[60:63], v[8:15], %[ix]\n" \
  "s_mov_b64 exec, %[vm]\n" \
  "ds_read_b128 v[60:63], %[va] offset:52736\n" \
  "s_mov_b64 exec, -1\n" \
  "v_exp_f32 v18, v18\n" \
  "v_exp_f32 v19, v19\n" \
  "buffer_load_dwordx4 v[220:223], %[ka], %[sw], %[w2t] offen offset:3072 sc1\n" \
  "v_exp_f32 v20, v20\n" \
  "v_exp_f32 v21, v21\n" \
  "s_waitcnt lgkmcnt(2)\n" \
  "v_mfma_f32_32x32x16_bf16 v[0:15], v[52:55], v[48:51], 0\n" \
  "ds_read_b128 v[52:55], %[ka] offset:1024\n" \
  "v_exp_f32 v22, v22\n" \
  "v_exp_f32 v23, v23\n" \
  "v_exp_f32 v24, v24\n" \
  "v_exp_f32 v25, v25\n" \
  "s_waitcnt lgkmcnt(2)\n" \
  "v_smfmac_f32_32x32x32_bf16 v[32:47], v[56:59], v[16:23], %[ix]\n" \
  "s_mov_b64 exec, %[vm]\n" \
  "ds_read_b128 v[56:59], %[va] offset:0\n" \
  "s_mov_b64 exec, -1\n" \
  "v_exp_f32 v26, v26\n" \
  "v_exp_f32 v27, v27\n" \
  "v_exp_f32 v28, v28\n" \
  "s_add_u32 m0, %[kvd], 32768\n" \
  "s_add_u32 %[t0], %[rot], %[kvs]\n" \
  "buffer_load_dwordx4 %[ka], %[sw], %[t0] offen offset:1024 sc1 lds\n" \
  "v_exp_f32 v29, v29\n" \
  "v_exp_f32 v30, v30\n" \
  "v_exp_f32 v31, v31\n" \
  "s_setprio 3\n" \
  "v_exp_f32 v0, v0\n" \
  "v_exp_f32 v1, v1\n" \
  "s_waitcnt lgkmcnt(2)\n" \
  "v_smfmac_f32_32x32x32_bf16 v[32:47], v[60:63], v[24:31], %[ix]\n" \
  "s_mov_b64 exec, %[vm]\n" \
  "ds_read_b128 v[60:63], %[va] offset:512\n" \
  "s_mov_b64 exec, 1\n" \
  "s_waitcnt vmcnt(6)\n" \
  "ds_add_u32 %[ca], %[ix]\n" \
  "s_mov_b64 exec, -1\n" \
  "v_exp_f32 v2, v2\n" \
  "v_exp_f32 v3, v3\n" \
  "s_add_u32 %[w2t], %[w2t], 4096\n" \
  "buffer_load_dwordx4 v[224:227], %[ka], %[sw], %[w2t] offen offset:0 sc1\n" \
  "v_exp_f32 v4, v4\n" \
  "v_exp_f32 v5, v5\n" \
  "s_waitcnt lgkmcnt(3)\n" \
  "v_mfma_f32_32x32x16_bf16 v[16:31], v[52:55], v[48:51], 0\n" \
  "ds_read_b128 v[52:55], %[ka] offset:2048\n" \
  "v_exp_f32 v6, v6\n" \
  "v_exp_f32 v7, v7\n" \
  "v_exp_f32 v8, v8\n" \
  "v_exp_f32 v9, v9\n" \
  "s_waitcnt lgkmcnt(3)\n" \
  "v_smfmac_f32_32x32x32_bf16 v[32:47], v[56:59], v[0:7], %[ix]\n" \
  "s_mov_b64 exec, %[vm]\n" \
  "ds_read_b128 v[56:59], %[va] offset:1024\n" \
  "s_mov_b64 exec, -1\n" \
  "v_exp_f32 v10, v10\n" \
  "v_exp_f32 v11, v11\n" \
  "v_exp_f32 v12, v12\n" \
  "v_exp_f32 v13, v13\n" \
  "v_exp_f32 v14, v14\n" \
  "v_exp_f32 v15, v15\n" \
  "s_setprio 2\n" \
  "v_exp_f32 v16, v16\n" \
  "v_exp_f32 v17, v17\n" \
  "s_waitcnt lgkmcnt(3)\n" \
  "v_smfmac_f32_32x32x32_bf16 v[32:47], v[60:63], v[8:15], %[ix]\n" \
  "s_mov_b64 exec, %[vm]\n" \
  "ds_read_b128 v[60:63], %[va] offset:1536\n" \
  "s_mov_b64 exec, -1\n" \
  "v_exp_f32 v18, v18\n" \
  "v_exp_f32 v19, v19\n" \
  "buffer_load_dwordx4 v[228:231], %[ka], %[sw], %[w2t] offen offset:1024 sc1\n" \
  "v_exp_f32 v20, v20\n" \
  "v_exp_f32 v21, v21\n" \
  "s_waitcnt lgkmcnt(2)\n" \
  "v_mfma_f32_32x32x16_bf16 v[0:15], v[52:55], v[48:51], 0\n" \
  "ds_read_b128 v[52:55], %[ka] offset:3072\n" \
  "v_exp_f32 v22, v22\n" \
  "v_exp_f32 v23, v23\n" \
  "v_exp_f32 v24, v24\n" \
  "v_exp_f32 v25, v25\n" \
  "s_waitcnt lgkmcnt(2)\n" \
  "v_smfmac_f32_32x32x32_bf16 v[32:47], v[56:59], v[16:23], %[ix]\n" \
  "s_mov_b64 exec, %[vm]\n" \
  "ds_read_b128 v[56:59], %[va] offset:2048\n" \
  "s_mov_b64 exec, -1\n" \
  "ds_read_b32 %[ct], %[ca]\n" \
  "v_exp_f32 v26, v26\n" \
  "v_exp_f32 v27, v27\n" \
  "v_exp_f32 v28, v28\n" \
  "v_exp_f32 v29, v29\n" \
  "v_exp_f32 v30, v30\n" \
  "v_exp_f32 v31, v31\n" \
  "s_setprio 1\n" \
  "v_exp_f32 v0, v0\n" \
  "v_exp_f32 v1, v1\n" \
  "s_waitcnt lgkmcnt(3)\n" \
  "v_smfmac_f32_32x32x32_bf16 v[32:47], v[60:63], v[24:31], %[ix]\n" \
  "s_mov_b64 exec, %[vm]\n" \
  "ds_read_b128 v[60:63], %[va] offset:2560\n" \
  "s_mov_b64 exec, -1\n" \
  "v_exp_f32 v2, v2\n" \
  "v_exp_f32 v3, v3\n" \
  "s_waitcnt lgkmcnt(1)\n" \
  "v_readfirstlane_b32 %[t1], %[ct]\n" \
  "s_cmp_ge_u32 %[t1], 2726256\n" \
  "s_cbranch_scc0 Lspin12_%=\n" \
  "Lback12_%=:\n" \
  "buffer_load_dwordx4 v[232:235], %[ka], %[sw], %[w2t] offen offset:2048 sc1\n" \
  "v_exp_f32 v4, v4\n" \
  "v_exp_f32 v5, v5\n" \
  "v_mfma_f32_32x32x16_bf16 v[16:31], v[52:55], v[48:51], 0\n" \
  "ds_read_b128 v[52:55], %[ka] offset:16384\n" \
  "v_exp_f32 v6, v6\n" \
  "v_exp_f32 v7, v7\n" \
  "v_exp_f32 v8, v8\n" \
  "v_exp_f32 v9, v9\n" \
  "v_smfmac_f32_32x32x32_bf16 v[32:47], v[56:59], v[0:7], %[ix]\n" \
  "s_mov_b64 exec, %[vm]\n" \
  "ds_read_b128 v[56:59], %[va] offset:3072\n" \
  "s_mov_b64 exec, -1\n" \
  "v_exp_f32 v10, v10\n" \
  "v_exp_f32 v11, v11\n" \
  "v_exp_f32 v12, v12\n" \
  "s_add_u32 %[rot], %[rot], 8192\n" \
  "s_and_b32 %[rot], %[rot], 0x1e000\n" \
  "s_add_u32 m0, %[kvd], 49152\n" \
  "s_add_u32 %[t0], %[rot], %[kvs]\n" \
  "buffer_load_dwordx4 %[ka], %[sw], %[t0] offen sc1 lds\n" \
  "v_exp_f32 v13, v13\n" \
  "v_exp_f32 v14, v14\n" \
  "v_exp_f32 v15, v15\n" \
  "s_setprio 0\n" \
  "v_exp_f32 v16, v16\n" \
  "v_exp_f32 v17, v17\n" \
  "s_waitcnt lgkmcnt(2)\n" \
  "v_smfmac_f32_32x32x32_bf16 v[32:47], v[60:63], v[8:15], %[ix]\n" \
  "s_mov_b64 exec, %[vm]\n" \
  "ds_read_b128 v[60:63], %[va] offset:3584\n" \
  "s_mov_b64 exec, -1\n" \
  "v_exp_f32 v18, v18\n" \
  "v_exp_f32 v19, v19\n" \
  "buffer_load_dwordx4 v[236:239], %[ka], %[sw], %[w2t] offen offset:3072 sc1\n" \
  "v_exp_f32 v20, v20\n" \
  "v_exp_f32 v21, v21\n" \
  "s_waitcnt lgkmcnt(2)\n" \
  "v_mfma_f32_32x32x16_bf16 v[0:15], v[52:55], v[48:51], 0\n" \
  "ds_read_b128 v[52:55], %[ka] offset:17408\n" \
  "v_exp_f32 v22, v22\n" \
  "v_exp_f32 v23, v23\n" \
  "v_exp_f32 v24, v24\n" \
  "v_exp_f32 v25, v25\n" \
  "s_waitcnt lgkmcnt(2)\n" \
  "v_smfmac_f32_32x32x32_bf16 v[32:47], v[56:59], v[16:23], %[ix]\n" \
  "s_mov_b64 exec, %[vm]\n" \
  "ds_read_b128 v[56:59], %[va] offset:16384\n" \
  "s_mov_b64 exec, -1\n" \
  "v_exp_f32 v26, v26\n" \
  "v_exp_f32 v27, v27\n" \
  "v_exp_f32 v28, v28\n" \
  "s_add_u32 m0, %[kvd], 49152\n" \
  "s_add_u32 %[t0], %[rot], %[kvs]\n" \
  "buffer_load_dwordx4 %[ka], %[sw], %[t0] offen offset:1024 sc1 lds\n" \
  "v_exp_f32 v29, v29\n" \
  "v_exp_f32 v30, v30\n" \
  "v_exp_f32 v31, v31\n" \
  "s_setprio 3\n" \
  "v_exp_f32 v0, v0\n" \
  "v_exp_f32 v1, v1\n" \
  "s_waitcnt lgkmcnt(2)\n" \
  "v_smfmac_f32_32x32x32_bf16 v[32:47], v[60:63], v[24:31], %[ix]\n" \
  "s_mov_b64 exec, %[vm]\n" \
  "ds_read_b128 v[60:63], %[va] offset:16896\n" \
  "s_mov_b64 exec, 1\n" \
  "s_waitcnt vmcnt(6)\n" \
  "ds_add_u32 %[ca], %[ix]\n" \
  "s_mov_b64 exec, -1\n" \
  "v_exp_f32 v2, v2\n" \
  "v_exp_f32 v3, v3\n" \
  "s_add_u32 m0, %[stg], 0\n" \
  "s_add_u32 %[t0], %[wot], 0\n" \
  "buffer_load_dwordx4 %[ka], %[sw], %[t0] offen offset:0 sc1 lds\n" \
  "v_exp_f32 v4, v4\n" \
  "v_exp_f32 v5, v5\n" \
  "s_waitcnt lgkmcnt(3)\n" \
  "v_mfma_f32_32x32x16_bf16 v[16:31], v[52:55], v[48:51], 0\n" \
  "ds_read_b128 v[52:55], %[ka] offset:18432\n" \
  "v_exp_f32 v6, v6\n" \
  "v_exp_f32 v7, v7\n" \
  "v_exp_f32 v8, v8\n" \
  "v_exp_f32 v9, v9\n" \
  "s_waitcnt lgkmcnt(3)\n" \
  "v_smfmac_f32_32x32x32_bf16 v[32:47], v[56:59], v[0:7], %[ix]\n" \
  "s_mov_b64 exec, %[vm]\n" \
  "ds_read_b128 v[56:59], %[va] offset:17408\n" \
  "s_mov_b64 exec, -1\n" \
  "v_exp_f32 v10, v10\n" \
  "v_exp_f32 v11, v11\n" \
  "v_exp_f32 v12, v12\n" \
  "v_exp_f32 v13, v13\n" \
  "v_exp_f32 v14, v14\n" \
  "v_exp_f32 v15, v15\n" \
  "s_setprio 2\n" \
  "v_exp_f32 v16, v16\n" \
  "v_exp_f32 v17, v17\n" \
  "s_waitcnt lgkmcnt(3)\n" \
  "v_smfmac_f32_32x32x32_bf16 v[32:47], v[60:63], v[8:15], %[ix]\n" \
  "s_mov_b64 exec, %[vm]\n" \
  "ds_read_b128 v[60:63], %[va] offset:17920\n" \
  "s_mov_b64 exec, -1\n" \
  "v_exp_f32 v18, v18\n" \
  "v_exp_f32 v19, v19\n" \
  "s_add_u32 m0, %[stg], 0\n" \
  "s_add_u32 %[t0], %[wot], 0\n" \
  "buffer_load_dwordx4 %[ka], %[sw], %[t0] offen offset:1024 sc1 lds\n" \
  "v_exp_f32 v20, v20\n" \
  "v_exp_f32 v21, v21\n" \
  "s_waitcnt lgkmcnt(2)\n" \
  "v_mfma_f32_32x32x16_bf16 v[0:15], v[52:55], v[48:51], 0\n" \
  "ds_read_b128 v[52:55], %[ka] offset:19456\n" \
  "v_exp_f32 v22, v22\n" \
  "v_exp_f32 v23, v23\n" \
  "v_exp_f32 v24, v24\n" \
  "v_exp_f32 v25, v25\n" \
  "s_waitcnt lgkmcnt(2)\n" \
  "v_smfmac_f32_32x32x32_bf16 v[32:47], v[56:59], v[16:23], %[ix]\n" \
  "s_mov_b64 exec, %[vm]\n" \
  "ds_read_b128 v[56:59], %[va] offset:18432\n" \
  "s_mov_b64 exec, -1\n" \
  "ds_read_b32 %[ct], %[ca]\n" \
  "v_exp_f32 v26, v26\n" \
  "v_exp_f32 v27, v27\n" \
  "v_exp_f32 v28, v28\n" \
  "v_exp_f32 v29, v29\n" \
  "v_exp_f32 v30, v30\n" \
  "v_exp_f32 v31, v31\n" \
  "s_setprio 1\n" \
  "v_exp_f32 v0, v0\n" \
  "v_exp_f32 v1, v1\n" \
  "s_waitcnt lgkmcnt(3)\n" \
  "v_smfmac_f32_32x32x32_bf16 v[32:47], v[60:63], v[24:31], %[ix]\n" \
  "s_mov_b64 exec, %[vm]\n" \
  "ds_read_b128 v[60:63], %[va] offset:18944\n" \
  "s_mov_b64 exec, -1\n" \
  "v_exp_f32 v2, v2\n" \
  "v_exp_f32 v3, v3\n" \
  "s_waitcnt lgkmcnt(1)\n" \
  "v_readfirstlane_b32 %[t1], %[ct]\n" \
  "s_cmp_ge_u32 %[t1], 2953444\n" \
  "s_cbranch_scc0 Lspin13_%=\n" \
  "Lback13_%=:\n" \
  "s_add_u32 m0, %[stg], 0\n" \
  "s_add_u32 %[t0], %[wot], 0\n" \
  "buffer_load_dwordx4 %[ka], %[sw], %[t0] offen offset:2048 sc1 lds\n" \
  "v_exp_f32 v4, v4\n" \
  "v_exp_f32 v5, v5\n" \
  "v_mfma_f32_32x32x16_bf16 v[16:31], v[52:55], v[48:51], 0\n" \
  "ds_read_b128 v[52:55], %[ka] offset:32768\n" \
  "v_exp_f32 v6, v6\n" \
  "v_exp_f32 v7, v7\n" \
  "v_exp_f32 v8, v8\n" \
  "v_exp_f32 v9, v9\n" \
  "v_smfmac_f32_32x32x32_bf16 v[32:47], v[56:59], v[0:7], %[ix]\n" \
  "s_mov_b64 exec, %[vm]\n" \
  "ds_read_b128 v[56:59], %[va] offset:19456\n" \
  "s_mov_b64 exec, -1\n" \
  "v_exp_f32 v10, v10\n" \
  "v_exp_f32 v11, v11\n" \
  "v_exp_f32 v12, v12\n" \
  "v_exp_f32 v13, v13\n" \
  "v_exp_f32 v14, v14\n" \
  "v_exp_f32 v15, v15\n" \
  "s_setprio 0\n" \
  "v_exp_f32 v16, v16\n" \
  "v_exp_f32 v17, v17\n" \
  "s_waitcnt lgkmcnt(2)\n" \
  "v_smfmac_f32_32x32x32_bf16 v[32:47], v[60:63], v[8:15], %[ix]\n" \
  "s_mov_b64 exec, %[vm]\n" \
  "ds_read_b128 v[60:63], %[va] offset:19968\n" \
  "s_mov_b64 exec, -1\n" \
  "v_exp_f32 v18, v18\n" \
  "v_exp_f32 v19, v19\n" \
  "s_add_u32 m0, %[stg], 0\n" \
  "s_add_u32 %[t0], %[wot], 0\n" \
  "buffer_load_dwordx4 %[ka], %[sw], %[t0] offen offset:3072 sc1 lds\n" \
  "v_exp_f32 v20, v20\n" \
  "v_exp_f32 v21, v21\n" \
  "s_waitcnt lgkmcnt(2)\n" \
  "v_mfma_f32_32x32x16_bf16 v[0:15], v[52:55], v[48:51], 0\n" \
  "ds_read_b128 v[52:55], %[ka] offset:33792\n" \
  "v_exp_f32 v22, v22\n" \
  "v_exp_f32 v23, v23\n" \
  "v_exp_f32 v24, v24\n" \
  "v_exp_f32 v25, v25\n" \
  "s_waitcnt lgkmcnt(2)\n" \
  "v_smfmac_f32_32x32x32_bf16 v[32:47], v[56:59], v[16:23], %[ix]\n" \
  "s_mov_b64 exec, %[vm]\n" \
  "ds_read_b128 v[56:59], %[va] offset:32768\n" \
  "s_mov_b64 exec, -1\n" \
  "v_exp_f32 v26, v26\n" \
  "v_exp_f32 v27, v27\n" \
  "v_exp_f32 v28, v28\n" \
  "v_exp_f32 v29, v29\n" \
  "v_exp_f32 v30, v30\n" \
  "v_exp_f32 v31, v31\n" \
  "s_setprio 3\n" \
  "v_exp_f32 v0, v0\n" \
  "v_exp_f32 v1, v1\n" \
  "s_waitcnt lgkmcnt(2)\n" \
  "v_smfmac_f32_32x32x32_bf16 v[32:47], v[60:63], v[24:31], %[ix]\n" \
  "s_mov_b64 exec, %[vm]\n" \
  "ds_read_b128 v[60:63], %[va] offset:33280\n" \
  "s_mov_b64 exec, 1\n" \
  "s_waitcnt vmcnt(4)\n" \
  "ds_add_u32 %[ca], %[ix]\n" \
  "s_mov_b64 exec, -1\n" \
  "v_exp_f32 v2, v2\n" \
  "v_exp_f32 v3, v3\n" \
  "s_add_u32 m0, %[stg], 4096\n" \
  "s_add_u32 %[t0], %[wot], 4096\n" \
  "buffer_load_dwordx4 %[ka], %[sw], %[t0] offen offset:0 sc1 lds\n" \
  "v_exp_f32 v4, v4\n" \
  "v_exp_f32 v5, v5\n" \
  "s_waitcnt lgkmcnt(3)\n" \
  "v_mfma_f32_32x32x16_bf16 v[16:31], v[52:55], v[48:51], 0\n" \
  "ds_read_b128 v[52:55], %[ka] offset:34816\n" \
  "v_exp_f32 v6, v6\n" \
  "v_exp_f32 v7, v7\n" \
  "v_exp_f32 v8, v8\n" \
  "v_exp_f32 v9, v9\n" \
  "s_waitcnt lgkmcnt(3)\n" \
  "v_smfmac_f32_32x32x32_bf16 v[32:47], v[56:59], v[0:7], %[ix]\n" \
  "s_mov_b64 exec, %[vm]\n" \
  "ds_read_b128 v[56:59], %[va] offset:33792\n" \
  "s_mov_b64 exec, -1\n" \
  "v_exp_f32 v10, v10\n" \
  "v_exp_f32 v11, v11\n" \
  "v_exp_f32 v12, v12\n" \
  "v_exp_f32 v13, v13\n" \
  "v_exp_f32 v14, v14\n" \
  "v_exp_f32 v15, v15\n" \
  "s_setprio 2\n" \
  "v_exp_f32 v16, v16\n" \
  "v_exp_f32 v17, v17\n" \
  "s_waitcnt lgkmcnt(3)\n" \
  "v_smfmac_f32_32x32x32_bf16 v[32:47], v[60:63], v[8:15], %[ix]\n" \
  "s_mov_b64 exec, %[vm]\n" \
  "ds_read_b128 v[60:63], %[va] offset:34304\n" \
  "s_mov_b64 exec, -1\n" \
  "v_exp_f32 v18, v18\n" \
  "v_exp_f32 v19, v19\n" \
  "s_add_u32 m0, %[stg], 4096\n" \
  "s_add_u32 %[t0], %[wot], 4096\n" \
  "buffer_load_dwordx4 %[ka], %[sw], %[t0] offen offset:1024 sc1 lds\n" \
  "v_exp_f32 v20, v20\n" \
  "v_exp_f32 v21, v21\n" \
  "s_waitcnt lgkmcnt(2)\n" \
  "v_mfma_f32_32x32x16_bf16 v[0:15], v[52:55], v[48:51], 0\n" \
  "ds_read_b128 v[52:55], %[ka] offset:35840\n" \
  "v_exp_f32 v22, v22\n" \
  "v_exp_f32 v23, v23\n" \
  "v_exp_f32 v24, v24\n" \
  "v_exp_f32 v25, v25\n" \
  "s_waitcnt lgkmcnt(2)\n" \
  "v_smfmac_f32_32x32x32_bf16 v[32:47], v[56:59], v[16:23], %[ix]\n" \
  "s_mov_b64 exec, %[vm]\n" \
  "ds_read_b128 v[56:59], %[va] offset:34816\n" \
  "s_mov_b64 exec, -1\n" \
  "ds_read_b32 %[ct], %[ca]\n" \
  "v_exp_f32 v26, v26\n" \
  "v_exp_f32 v27, v27\n" \
  "v_exp_f32 v28, v28\n" \
  "v_exp_f32 v29, v29\n" \
  "v_exp_f32 v30, v30\n" \
  "v_exp_f32 v31, v31\n" \
  "s_setprio 1\n" \
  "v_exp_f32 v0, v0\n" \
  "v_exp_f32 v1, v1\n" \
  "s_waitcnt lgkmcnt(3)\n" \
  "v_smfmac_f32_32x32x32_bf16 v[32:47], v[60:63], v[24:31], %[ix]\n" \
  "s_mov_b64 exec, %[vm]\n" \
  "ds_read_b128 v[60:63], %[va] offset:35328\n" \
  "s_mov_b64 exec, -1\n" \
  "v_exp_f32 v2, v2\n" \
  "v_exp_f32 v3, v3\n" \
  "s_waitcnt lgkmcnt(1)\n" \
  "v_readfirstlane_b32 %[t1], %[ct]\n" \
  "s_cmp_ge_u32 %[t1], 3180632\n" \
  "s_cbranch_scc0 Lspin14_%=\n" \
  "Lback14_%=:\n" \
  "s_add_u32 m0, %[stg], 4096\n" \
  "s_add_u32 %[t0], %[wot], 4096\n" \
  "buffer_load_dwordx4 %[ka], %[sw], %[t0] offen offset:2048 sc1 lds\n" \
  "v_exp_f32 v4, v4\n" \
  "v_exp_f32 v5, v5\n" \
  "v_mfma_f32_32x32x16_bf16 v[16:31], v[52:55], v[48:51], 0\n" \
  "ds_read_b128 v[52:55], %[ka] offset:49152\n" \
  "v_exp_f32 v6, v6\n" \
  "v_exp_f32 v7, v7\n" \
  "v_exp_f32 v8, v8\n" \
  "v_exp_f32 v9, v9\n" \
  "v_smfmac_f32_32x32x32_bf16 v[32:47], v[56:59], v[0:7], %[ix]\n" \
  "s_mov_b64 exec, %[vm]\n" \
  "ds_read_b128 v[56:59], %[va] offset:35840\n" \
  "s_mov_b64 exec, -1\n" \
  "v_exp_f32 v10, v10\n" \
  "v_exp_f32 v11, v11\n" \
  "v_exp_f32 v12, v12\n" \
  "v_exp_f32 v13, v13\n" \
  "v_exp_f32 v14, v14\n" \
  "v_exp_f32 v15, v15\n" \
  "s_setprio 0\n" \
  "v_exp_f32 v16, v16\n" \
  "v_exp_f32 v17, v17\n" \
  "s_waitcnt lgkmcnt(2)\n" \
  "v_smfmac_f32_32x32x32_bf16 v[32:47], v[60:63], v[8:15], %[ix]\n" \
  "s_mov_b64 exec, %[vm]\n" \
  "ds_read_b128 v[60:63], %[va] offset:36352\n" \
  "s_mov_b64 exec, -1\n" \
  "v_exp_f32 v18, v18\n" \
  "v_exp_f32 v19, v19\n" \
  "s_add_u32 m0, %[stg], 4096\n" \
  "s_add_u32 %[t0], %[wot], 4096\n" \
  "buffer_load_dwordx4 %[ka], %[sw], %[t0] offen offset:3072 sc1 lds\n" \
  "v_exp_f32 v20, v20\n" \
  "v_exp_f32 v21, v21\n" \
  "s_waitcnt lgkmcnt(2)\n" \
  "v_mfma_f32_32x32x16_bf16 v[0:15], v[52:55], v[48:51], 0\n" \
  "ds_read_b128 v[52:55], %[ka] offset:50176\n" \
  "v_exp_f32 v22, v22\n" \
  "v_exp_f32 v23, v23\n" \
  "v_exp_f32 v24, v24\n" \
  "v_exp_f32 v25, v25\n" \
  "s_waitcnt lgkmcnt(2)\n" \
  "v_smfmac_f32_32x32x32_bf16 v[32:47], v[56:59], v[16:23], %[ix]\n" \
  "s_mov_b64 exec, %[vm]\n" \
  "ds_read_b128 v[56:59], %[va] offset:49152\n" \
  "s_mov_b64 exec, -1\n" \
  "v_exp_f32 v26, v26\n" \
  "v_exp_f32 v27, v27\n" \
  "v_exp_f32 v28, v28\n" \
  "v_exp_f32 v29, v29\n" \
  "v_exp_f32 v30, v30\n" \
  "v_exp_f32 v31, v31\n" \
  "s_setprio 3\n" \
  "v_exp_f32 v0, v0\n" \
  "v_exp_f32 v1, v1\n" \
  "s_waitcnt lgkmcnt(2)\n" \
  "v_smfmac_f32_32x32x32_bf16 v[32:47], v[60:63], v[24:31], %[ix]\n" \
  "s_mov_b64 exec, %[vm]\n" \
  "ds_read_b128 v[60:63], %[va] offset:49664\n" \
  "s_mov_b64 exec, -1\n" \
  "v_exp_f32 v2, v2\n" \
  "v_exp_f32 v3, v3\n" \
  "v_exp_f32 v4, v4\n" \
  "v_exp_f32 v5, v5\n" \
  "s_waitcnt lgkmcnt(2)\n" \
  "v_mfma_f32_32x32x16_bf16 v[16:31], v[52:55], v[48:51], 0\n" \
  "ds_read_b128 v[52:55], %[ka] offset:51200\n" \
  "v_exp_f32 v6, v6\n" \
  "v_exp_f32 v7, v7\n" \
  "v_exp_f32 v8, v8\n" \
  "v_exp_f32 v9, v9\n" \
  "s_waitcnt lgkmcnt(2)\n" \
  "v_smfmac_f32_32x32x32_bf16 v[32:47], v[56:59], v[0:7], %[ix]\n" \
  "s_mov_b64 exec, %[vm]\n" \
  "ds_read_b128 v[56:59], %[va] offset:50176\n" \
  "s_mov_b64 exec, -1\n" \
  "v_exp_f32 v10, v10\n" \
  "v_exp_f32 v11, v11\n" \
  "v_exp_f32 v12, v12\n" \
  "v_exp_f32 v13, v13\n" \
  "v_exp_f32 v14, v14\n" \
  "v_exp_f32 v15, v15\n" \
  "s_setprio 2\n" \
  "v_exp_f32 v16, v16\n" \
  "v_exp_f32 v17, v17\n" \
  "s_waitcnt lgkmcnt(2)\n" \
  "v_smfmac_f32_32x32x32_bf16 v[32:47], v[60:63], v[8:15], %[ix]\n" \
  "s_mov_b64 exec, %[vm]\n" \
  "ds_read_b128 v[60:63], %[va] offset:50688\n" \
  "s_mov_b64 exec, -1\n" \
  "v_exp_f32 v18, v18\n" \
  "v_exp_f32 v19, v19\n" \
  "v_exp_f32 v20, v20\n" \
  "v_exp_f32 v21, v21\n" \
  "s_waitcnt lgkmcnt(2)\n" \
  "v_mfma_f32_32x32x16_bf16 v[0:15], v[52:55], v[48:51], 0\n" \
  "ds_read_b128 v[52:55], %[ka] offset:52224\n" \
  "v_exp_f32 v22, v22\n" \
  "v_exp_f32 v23, v23\n" \
  "v_exp_f32 v24, v24\n" \
  "v_exp_f32 v25, v25\n" \
  "s_waitcnt lgkmcnt(2)\n" \
  "v_smfmac_f32_32x32x32_bf16 v[32:47], v[56:59], v[16:23], %[ix]\n" \
  "s_mov_b64 exec, %[vm]\n" \
  "ds_read_b128 v[56:59], %[va] offset:51200\n" \
  "s_mov_b64 exec, -1\n" \
  "v_exp_f32 v26, v26\n" \
  "v_exp_f32 v27, v27\n" \
  "v_exp_f32 v28, v28\n" \
  "v_exp_f32 v29, v29\n" \
  "v_exp_f32 v30, v30\n" \
  "v_exp_f32 v31, v31\n" \
  "s_setprio 1\n" \
  "v_exp_f32 v0, v0\n" \
  "v_exp_f32 v1, v1\n" \
  "s_waitcnt lgkmcnt(2)\n" \
  "v_smfmac_f32_32x32x32_bf16 v[32:47], v[60:63], v[24:31], %[ix]\n" \
  "s_mov_b64 exec, %[vm]\n" \
  "ds_read_b128 v[60:63], %[va] offset:51712\n" \
  "s_mov_b64 exec, -1\n" \
  "v_exp_f32 v2, v2\n" \
  "v_exp_f32 v3, v3\n" \
  "v_exp_f32 v4, v4\n" \
  "v_exp_f32 v5, v5\n" \
  "s_waitcnt lgkmcnt(2)\n" \
  "v_mfma_f32_32x32x16_bf16 v[16:31], v[52:55], v[48:51], 0\n" \
  "ds_read_b128 v[52:55], %[ka] offset:0\n" \
  "v_exp_f32 v6, v6\n" \
  "v_exp_f32 v7, v7\n" \
  "v_exp_f32 v8, v8\n" \
  "v_exp_f32 v9, v9\n" \
  "s_waitcnt lgkmcnt(2)\n" \
  "v_smfmac_f32_32x32x32_bf16 v[32:47], v[56:59], v[0:7], %[ix]\n" \
  "s_mov_b64 exec, %[vm]\n" \
  "ds_read_b128 v[56:59], %[va] offset:52224\n" \
  "s_mov_b64 exec, -1\n" \
  "v_exp_f32 v10, v10\n" \
  "v_exp_f32 v11, v11\n" \
  "v_exp_f32 v12, v12\n" \
  "v_exp_f32 v13, v13\n" \
  "v_exp_f32 v14, v14\n" \
  "v_exp_f32 v15, v15\n" \
  "s_setprio 0\n" \
  "v_exp_f32 v16, v16\n" \
  "v_exp_f32 v17, v17\n" \
  "s_waitcnt lgkmcnt(2)\n" \
  "v_smfmac_f32_32x32x32_bf16 v[32:47], v[60:63], v[8:15], %[ix]\n" \
  "s_mov_b64 exec, %[vm]\n" \
  "ds_read_b128 v[60:63], %[va] offset:52736\n" \
  "s_mov_b64 exec, -1\n" \
  "v_exp_f32 v18, v18\n" \
  "v_exp_f32 v19, v19\n" \
  "v_exp_f32 v20, v20\n" \
  "v_exp_f32 v21, v21\n" \
  "s_waitcnt lgkmcnt(2)\n" \
  "v_mfma_f32_32x32x16_bf16 v[0:15], v[52:55], v[48:51], 0\n" \
  "ds_read_b128 v[52:55], %[ka] offset:1024\n" \
  "v_exp_f32 v22, v22\n" \
  "v_exp_f32 v23, v23\n" \
  "v_exp_f32 v24, v24\n" \
  "v_exp_f32 v25, v25\n" \
  "s_waitcnt lgkmcnt(2)\n" \
  "v_smfmac_f32_32x32x32_bf16 v[32:47], v[56:59], v[16:23], %[ix]\n" \
  "s_mov_b64 exec, %[vm]\n" \
  "ds_read_b128 v[56:59], %[va] offset:0\n" \
  "s_mov_b64 exec, -1\n" \
  "v_exp_f32 v26, v26\n" \
  "v_exp_f32 v27, v27\n" \
  "v_exp_f32 v28, v28\n" \
  "v_exp_f32 v29, v29\n" \
  "v_exp_f32 v30, v30\n" \
  "v_exp_f32 v31, v31\n" \
  "s_setprio 0\n" \
  "s_waitcnt vmcnt(0) lgkmcnt(0)\n" \
  "s_nop 1\n" \
  "v_smfmac_f32_32x32x32_bf16 v[32:47], v[60:63], v[24:31], %[ix]\n" \
  "s_branch Lend_%=\n" \
  "Lspin1_%=:\n" \
  "s_sleep 1\n" \
  "ds_read_b32 %[ct], %[ca]\n" \
  "s_waitcnt lgkmcnt(0)\n" \
  "v_readfirstlane_b32 %[t1], %[ct]\n" \
  "s_cmp_ge_u32 %[t1], 227188\n" \
  "s_cbranch_scc0 Lspin1_%=\n" \
  "s_branch Lback1_%=\n" \
  "Lspin2_%=:\n" \
  "s_sleep 1\n" \
  "ds_read_b32 %[ct], %[ca]\n" \
  "s_waitcnt lgkmcnt(0)\n" \
  "v_readfirstlane_b32 %[t1], %[ct]\n" \
  "s_cmp_ge_u32 %[t1], 454376\n" \
  "s_cbranch_scc0 Lspin2_%=\n" \
  "s_branch Lback2_%=\n" \
  "Lwsp_%=:\n" \
  "s_mov_b32 %[t1], 0x100000\n" \
  "Lwsq_%=:\n" \
  "s_sleep 2\n" \
  "buffer_load_dwordx4 v[236:239], %[ka], %[sw], %[f5] offen sc1\n" \
  "s_waitcnt vmcnt(0)\n" \
  "v_cmp_ne_u32 vcc, %[tag], v236\n" \
  "v_cmp_ne_u32 %[p0], %[tag], v237\n" \
  "s_or_b64 vcc, vcc, %[p0]\n" \
  "v_cmp_ne_u32 %[p0], %[tag], v238\n" \
  "s_or_b64 vcc, vcc, %[p0]\n" \
  "v_cmp_ne_u32 %[p0], %[tag], v239\n" \
  "s_or_b64 vcc, vcc, %[p0]\n" \
  "s_cbranch_scc0 Lwok_%=\n" \
  "s_sub_u32 %[t1], %[t1], 1\n" \
  "s_cbranch_scc0 Lwsq_%=\n" \
  "s_branch Lwok_%=\n" \
  "Lspin3_%=:\n" \
  "s_sleep 1\n" \
  "ds_read_b32 %[ct], %[ca]\n" \
  "s_waitcnt lgkmcnt(0)\n" \
  "v_readfirstlane_b32 %[t1], %[ct]\n" \
  "s_cmp_ge_u32 %[t1], 681564\n" \
  "s_cbranch_scc0 Lspin3_%=\n" \
  "s_branch Lback3_%=\n" \
  "Lspin4_%=:\n" \
  "s_sleep 1\n" \
  "ds_read_b32 %[ct], %[ca]\n" \
  "s_waitcnt lgkmcnt(0)\n" \
  "v_readfirstlane_b32 %[t1], %[ct]\n" \
  "s_cmp_ge_u32 %[t1], 908752\n" \
  "s_cbranch_scc0 Lspin4_%=\n" \
  "s_branch Lback4_%=\n" \
  "Lspin5_%=:\n" \
  "s_sleep 1\n" \
  "ds_read_b32 %[ct], %[ca]\n" \
  "s_waitcnt lgkmcnt(0)\n" \
  "v_readfirstlane_b32 %[t1], %[ct]\n" \
  "s_cmp_ge_u32 %[t1], 1135940\n" \
  "s_cbranch_scc0 Lspin5_%=\n" \
  "s_branch Lback5_%=\n" \
  "Lspin6_%=:\n" \
  "s_sleep 1\n" \
  "ds_read_b32 %[ct], %[ca]\n" \
  "s_waitcnt lgkmcnt(0)\n" \
  "v_readfirstlane_b32 %[t1], %[ct]\n" \
  "s_cmp_ge_u32 %[t1], 1363128\n" \
  "s_cbranch_scc0 Lspin6_%=\n" \
  "s_branch Lback6_%=\n" \
  "Lspin7_%=:\n" \
  "s_sleep 1\n" \
  "ds_read_b32 %[ct], %[ca]\n" \
  "s_waitcnt lgkmcnt(0)\n" \
  "v_readfirstlane_b32 %[t1], %[ct]\n" \
  "s_cmp_ge_u32 %[t1], 1590316\n" \
  "s_cbranch_scc0 Lspin7_%=\n" \
  "s_branch Lback7_%=\n" \
  "Lspin8_%=:\n" \
  "s_sleep 1\n" \
  "ds_read_b32 %[ct], %[ca]\n" \
  "s_waitcnt lgkmcnt(0)\n" \
  "v_readfirstlane_b32 %[t1], %[ct]\n" \
  "s_cmp_ge_u32 %[t1], 1817504\n" \
  "s_cbranch_scc0 Lspin8_%=\n" \
  "s_branch Lback8_%=\n" \
  "Lspin9_%=:\n" \
  "s_sleep 1\n" \
  "ds_read_b32 %[ct], %[ca]\n" \
  "s_waitcnt lgkmcnt(0)\n" \
  "v_readfirstlane_b32 %[t1], %[ct]\n" \
  "s_cmp_ge_u32 %[t1], 2044692\n" \
  "s_cbranch_scc0 Lspin9_%=\n" \
  "s_branch Lback9_%=\n" \
  "Lspin10_%=:\n" \
  "s_sleep 1\n" \
  "ds_read_b32 %[ct], %[ca]\n" \
  "s_waitcnt lgkmcnt(0)\n" \
  "v_readfirstlane_b32 %[t1], %[ct]\n" \
  "s_cmp_ge_u32 %[t1], 2271880\n" \
  "s_cbranch_scc0 Lspin10_%=\n" \
  "s_branch Lback10_%=\n" \
  "Lspin11_%=:\n" \
  "s_sleep 1\n" \
  "ds_read_b32 %[ct], %[ca]\n" \
  "s_waitcnt lgkmcnt(0)\n" \
  "v_readfirstlane_b32 %[t1], %[ct]\n" \
  "s_cmp_ge_u32 %[t1], 2499068\n" \
  "s_cbranch_scc0 Lspin11_%=\n" \
  "s_branch Lback11_%=\n" \
  "Lspin12_%=:\n" \
  "s_sleep 1\n" \
  "ds_read_b32 %[ct], %[ca]\n" \
  "s_waitcnt lgkmcnt(0)\n" \
  "v_readfirstlane_b32 %[t1], %[ct]\n" \
  "s_cmp_ge_u32 %[t1], 2726256\n" \
  "s_cbranch_scc0 Lspin12_%=\n" \
  "s_branch Lback12_%=\n" \
  "Lspin13_%=:\n" \
  "s_sleep 1\n" \
  "ds_read_b32 %[ct], %[ca]\n" \
  "s_waitcnt lgkmcnt(0)\n" \
  "v_readfirstlane_b32 %[t1], %[ct]\n" \
  "s_cmp_ge_u32 %[t1], 2953444\n" \
  "s_cbranch_scc0 Lspin13_%=\n" \
  "s_branch Lback13_%=\n" \
  "Lspin14_%=:\n" \
  "s_sleep 1\n" \
  "ds_read_b32 %[ct], %[ca]\n" \
  "s_waitcnt lgkmcnt(0)\n" \
  "v_readfirstlane_b32 %[t1], %[ct]\n" \
  "s_cmp_ge_u32 %[t1], 3180632\n" \
  "s_cbranch_scc0 Lspin14_%=\n" \
  "s_branch Lback14_%=\n" \
  "Lend_%=:\n" \
  "s_nop 7\n" \
  "s_nop 7\n"

#define CLK "s_memrealtime"
#define DS(k, wv)
#define STAMP(k)

__global__ __launch_bounds__(512, 2) void fused_block(const u16* __restrict__ x, const u16* __restrict__ wqkv, const u16* __restrict__ wo, const u16* __restrict__ w1,
                                                     const u16* __restrict__ w2, u16* __restrict__ out, char* ws) {
  __shared__ __attribute__((aligned(1024))) char smem[163840];
  STAMP(0);
  const int w = __builtin_amdgcn_readfirstlane(threadIdx.x >> 6), t = w & 3, j = w >> 2;
  const int b = blockIdx.x, xcd = b & 7, bi = b >> 3;
  const int hp = (xcd & 3) * 4 + (bi >> 3), rt = (xcd >> 2) * 8 + (bi & 7);
  const int rg = xcd * 4 + (bi >> 3), cs = bi & 7;
  const unsigned lds0 = (unsigned)(size_t)smem;
  const srd_t sx = __builtin_amdgcn_make_buffer_rsrc((void*)x, 0, 2048 * 512 * 2, 0x00020000);
  const srd_t sq = __builtin_amdgcn_make_buffer_rsrc((void*)wqkv, 0, 512 * 1536 * 2, 0x00020000);
  const srd_t sw = __builtin_amdgcn_make_buffer_rsrc((void*)ws, 0, WS_END, 0x00020000);
  const bool w7 = (w == 7);
  asm volatile("" ::"s"(wo), "s"(w1), "s"(w2), "s"(out), "s"(ws));
  unsigned tag;
  f32x16 OA;
  u32x4 Qf;
  u32x16 W1r[8], W2r[3];
  bool bad;
  const unsigned w2t0 = WS_W2 + (cs * 112 + 14 * w) * 2048;
  {
    const int l = threadIdx.x & 63, r = l & 31, hh = l >> 5, blk = (l >> 4) & 1, q4 = (l & 15) >> 2, p4 = l & 3;
    const unsigned vx0 = (l >> 3) * 1024 + (((l & 7) ^ (l >> 4)) << 4), vx1 = (vx0 ^ 64) + 8192;
    const unsigned vw0 = (l >> 2) * 3072 + (p4 << 4), vw1 = vw0 ^ 32;
    const unsigned sxo = (rt * 128 + 32 * t) * 1024, swo = t * 16 * 3072 + hp * 64;
#define CROT 1
    const int crot = CROT == 0 ? 0 : CROT == 1 ? bi + (bi >> 3) * 2 : CROT == 2 ? ((bi + (bi >> 3)) & 1) * 4 : CROT == 3 ? ((bi + (bi >> 3)) & 3) * 2 : CROT == 4 ? xcd : CROT == 5 ? xcd + ((bi + (bi >> 3)) & 1) * 4 : ((bi + (bi >> 3)) & 3);
    auto p1_piece = [&](int cc, int i) {
      const int c = (cc + crot) & 7;
      if (i < 4) dma(lds0 + (cc & 3) * 16384 + t * 4096 + i * 1024, (i & 1) ? vx1 : vx0, sx, sxo + (i >> 1) * 16384 + c * 128);
      else dma(lds0 + 65536 + (cc & 3) * 12288 + t * 3072 + (i - 4) * 1024, i == 5 ? vw1 : vw0, sq, swo + (i - 4) * 1024 + c * 196608);
    };
    unsigned ep = 0, cpfx = 0;
    float lnpub = 0.f;
    const int lrow = rt * 128 + hp * 8 + t * 2;
    if (j == 1) {
      DS(0, 4);
#pragma unroll
      for (int n = 0; n < LEAD; n++) { p1_piece(n / 7, n % 7); if (n == 6) DS(1, 4); }
      DS(2, 4);
    } else {
      u32x4 u0, u1;
      cpfx = (bi * 16 + t * 4 + (l & 3)) * 128;
      cpfx = cpfx < CODE_BYTES ? cpfx : 0;
      unsigned long long pc;
      asm volatile("buffer_load_dwordx4 %0, %5, %6, %7 offen\n\tbuffer_load_dwordx4 %1, %5, %6, %7 offen offset:1024\n\tbuffer_load_dword %2, off, %8, 0 sc1\n\t"
                   "s_getpc_b64 %4\n\tglobal_load_dword %3, %3, %4\n\ts_waitcnt vmcnt(2)"
                   : "=&v"(u0), "=&v"(u1), "=&v"(ep), "+v"(cpfx), "=&s"(pc) : "v"(l * 16), "s"(sx), "s"(lrow * 1024), "s"(sw) : "memory");
      DS(3, 0);
      const bf16x8 f0 = __builtin_bit_cast(bf16x8, u0), f1 = __builtin_bit_cast(bf16x8, u1);
      const float m0 = wave_sum(dot_ones(f0, 0.f)) * (1.f / 512.f), m1 = wave_sum(dot_ones(f1, 0.f)) * (1.f / 512.f);
      const float e0 = wave_sum(dot_self(f0, 0.f)) * (1.f / 512.f), e1 = wave_sum(dot_self(f1, 0.f)) * (1.f / 512.f);
      const float m = (l & 2) ? m1 : m0, e = (l & 2) ? e1 : e0;
      const float ra = __builtin_amdgcn_rsqf(fmaxf(e - m * m, 0.f) + 1e-5f);
      lnpub = (l & 1) ? -m : ra;
      asm volatile("" : "+v"(lnpub));
      DS(4, 0);
    }
    const int tv = (b - 32) * 8 + w, tks = b < 32 ? b : b < 144 ? tv / 28 : b - 144, tpr = (b >= 32 && b < 144) ? tv - tks * 28 : w;
    const unsigned tstr = (b >= 32 && b < 144) ? 3584 : 1024;
    const srd_t st = __builtin_amdgcn_make_buffer_rsrc((void*)(b < 32 ? wo : b < 144 ? w1 : w2), 0, b < 32 ? 512 * 512 * 2 : 1792 * 512 * 2, 0x00020000);
    const unsigned vt = (l >> 3) * tstr + (((l & 7) ^ (blk << 2)) << 4), tso = 16 * tks * tstr + tpr * 128;
    auto tail_issue = [&]() { dma(lds0 + LW + w * 2048, vt, st, tso); dma(lds0 + LW + w * 2048 + 1024, vt, st, tso + 8 * tstr); };
    f32x16 aQK, aV;
#pragma unroll
    for (int i = 0; i < 16; i++) { aQK[i] = 0.f; aV[i] = 0.f; }
    float cqk = 0.f, cv = 0.f;
    {
      unsigned xa[4], qa[4], va[4];
#pragma unroll
      for (int i = 0; i < 4; i++) {
        const int ks = (i + t) & 3;
        xa[i] = lds0 + (32 * t + r) * 128 + (((2 * ks + hh) ^ ((r >> 1) & 7)) << 4);
        qa[i] = lds0 + 65536 + ks * 3072 + blk * 1024 + (8 * hh + q4) * 64 + (j ^ blk) * 32 + p4 * 8;
        va[i] = lds0 + 65536 + ks * 3072 + 2048 + (8 * hh + q4) * 64 + blk * 32 + p4 * 8;
        asm volatile("" : "+v"(xa[i]), "+v"(qa[i]), "+v"(va[i]));
      }
#define PINF(x) asm volatile("" : "+v"(x))
      if (j == 0) {
        bf16x8 xf[2][4], wa[2][4], wv[2][4];
        u32x2 lnv;
        const unsigned lno = WS_LN + (rt * 128 + 32 * t) * 16, lnl = r * 16 + hh * 8;
        auto rd = [&](int c, int i) {
          wa[c & 1][i] = trf(qa[i] + (c & 3) * 12288, 256);
          xf[c & 1][i] = ldsf(xa[i] + (c & 3) * 16384);
          wv[c & 1][i] = trf(va[i] + (c & 3) * 12288, 256);
        };
#pragma unroll
        for (int c = 0; c <= 8; c++) {
          const int s = c & 1, p = s ^ 1;
          if (c < 8) BARL();
#pragma unroll
          for (int i = 0; i < 4; i++) {
            if (c < 8) { rd(c, i); SB0(); }
            if (c > 0) { if (i & 1) aV = MFMA(xf[p][2 + (i >> 1)], wv[p][2 + (i >> 1)], aV); else aQK = MFMA(wa[p][2 + (i >> 1)], xf[p][2 + (i >> 1)], aQK); SB0(); }
          }
          if (c == 0) {
            asm volatile("s_waitcnt vmcnt(0)" : "+v"(ep), "+v"(cpfx)::"memory");
            if (l < 4) __hip_atomic_store((GAS unsigned long long*)(ws + WS_LN) + lrow * 2 + l, ((unsigned long long)(ep + 1u) << 32) | __float_as_uint(lnpub), RLX);
          }
          if (c < 8) {
            if (c == TCH) { asm volatile("buffer_load_dwordx2 %0, %1, %2, %3 offen sc1" : "=v"(lnv) : "v"(lnl), "s"(sw), "s"(lno) : "memory"); tail_issue(); }
            cqk = dot_ones(wa[s][0], cqk); cv = dot_ones(wv[s][0], cv); PINF(cqk); PINF(cv);
            SB0();
#pragma unroll
            for (int i = 0; i < 4; i++) {
              if (i & 1) aV = MFMA(xf[s][i >> 1], wv[s][i >> 1], aV); else aQK = MFMA(wa[s][i >> 1], xf[s][i >> 1], aQK);
              SB0();
            }
          }
        }
        asm volatile("s_waitcnt vmcnt(2)" : "+v"(lnv)::"memory");
#pragma unroll 1
        for (unsigned sp = 0; sp < SPIN_CAP && !__all(lnv[1] == ep + 1u); ++sp)
          asm volatile("buffer_load_dwordx2 %0, %1, %2, %3 offen sc1\n\ts_waitcnt vmcnt(0)" : "=&v"(lnv) : "v"(lnl), "s"(sw), "s"(lno) : "memory");
        *(LAS unsigned*)(size_t)(lds0 + T_A + hh * 512 + (32 * t + r) * 4) = lnv[0];
        *(LAS unsigned*)(size_t)(lds0 + T_TAG) = ep + 1u;
      } else {
        bf16x8 xf[4], wa[4];
#define PIECE(n) do { if ((n) < 56) p1_piece((n) / 7, (n) % 7); } while (0)
#pragma unroll
        for (int c = 0; c < 8; c++) {
          const int n0 = LEAD + 7 * c;
          asm volatile("s_waitcnt vmcnt(%0) lgkmcnt(0)\n\ts_barrier" ::"n"((n0 < 56 ? n0 : 56) - 7 * c - 7 + (c > TCH ? 2 : 0)) : "memory");
          if (c == 0) DS(5, 4);
          if (c == 4) DS(6, 4);
          if (c == 7) DS(7, 4);
          PIECE(n0);
#pragma unroll
          for (int i = 0; i < 4; i++) { xf[i] = ldsf(xa[i] + (c & 3) * 16384); wa[i] = trf(qa[i] + (c & 3) * 12288, 256); SB0(); }
          PIECE(n0 + 1); PIECE(n0 + 2);
          cqk = dot_ones(wa[0], cqk); PINF(cqk);
          SB0();
#pragma unroll
          for (int i = 0; i < 4; i++) {
            aQK = MFMA(wa[i], xf[i], aQK); SB0();
            PIECE(n0 + 3 + i);
          }
          if (c == TCH) tail_issue();
        }
      }
    }
    STAMP(1);
    *(LAS float*)(size_t)(lds0 + T_CS + j * 1024 + t * 256 + l * 4) = cqk;
    if (j == 0) *(LAS float*)(size_t)(lds0 + T_CS + 2048 + t * 256 + l * 4) = cv;
    BARL();
    tag = __builtin_amdgcn_readfirstlane(*(LAS unsigned*)(size_t)(lds0 + T_TAG));
    float qn = 0.f, kn = 0.f;
    {
      const float ra = lds1(lds0 + T_A + (32 * t + r) * 4), nm = lds1(lds0 + T_B + (32 * t + r) * 4);
      const unsigned ca = lds0 + T_CS + j * 1024 + l * 4;
      aQK = MFMA32((lds1(ca) + lds1(ca + 256)) + (lds1(ca + 512) + lds1(ca + 768)), nm, aQK);
      const unsigned rec = WS_KV + (hp * 32 + rt) * 8192;
      if (j == 0) {
        aV = MFMA32(nm, (lds1(ca + 2048) + lds1(ca + 2304)) + (lds1(ca + 2560) + lds1(ca + 2816)), aV);
      }
      float qk[16];
      const float rq = ra * (0.25f * 1.4426950408889634f);
#pragma unroll
      for (int i = 0; i < 8; i++) qk[8 + i] = ra * aQK[8 + i];
      const u32x4 Kp = pk8(qk + 8);
      const unsigned ko = t * 1024 + hh * 512 + ((r & 19) | ((r & 4) << 1) | ((r & 8) >> 1)) * 16;
      __builtin_amdgcn_raw_buffer_store_b128(Kp, sw, ko, rec + j * 131072, 16);
      *(LAS u32x4*)(size_t)(lds0 + LA + j * 8192 + ko) = Kp;
      if (j == 0) {
        float vv[16];
#pragma unroll
        for (int g = 0; g < 4; g++) {
          f32x4 a4 = lds4(lds0 + T_A + t * 128 + g * 32 + hh * 16);
#pragma unroll
          for (int e = 0; e < 4; e++) vv[4 * g + e] = a4[e] * aV[4 * g + e];
        }
        const unsigned vo = 4096 + t * 1024 + hh * 256 + (l & 15) * 16;
#pragma unroll
        for (int s = 0; s < 2; s++) {
          const u32x4 Vp = pk8(vv + 8 * s);
          __builtin_amdgcn_raw_buffer_store_b128(Vp, sw, vo + blk * 131072 + s * 512, rec, 16);
          *(LAS u32x4*)(size_t)(lds0 + LA + blk * 8192 + vo + s * 512) = Vp;
        }
      }
#pragma unroll
      for (int i = 0; i < 8; i++) { qk[i] = rq * aQK[i]; qn = fmaf(qk[i], qk[i], qn); kn = fmaf(qk[8 + i], qk[8 + i], kn); }
      Qf = pk8(qk);
    }
    BARL();
    *(LAS float*)(size_t)(lds0 + T_KN + j * 1024 + hh * 512 + t * 128 + r * 4) = kn;
    if (threadIdx.x < 2) *(LAS unsigned*)(size_t)(lds0 + T_CNT + threadIdx.x * 4) = 0u;
    STAMP(2);

    f32x16 SA, SB;
    const unsigned one = (r == 16 || r == 20) ? 0x3f803f80u : 0u;
    u32x4 Kf = {0, 0, 0, 0}, V0 = {one, one, one, one}, V1 = V0;
#pragma unroll
    for (int i = 0; i < 16; i++) { SA[i] = 0.f; SB[i] = 0.f; OA[i] = 0.f; }
    const unsigned sidx = 0xDDDDu, kaddr = lds0 + LA + j * 8192 + l * 16, vaddr = lds0 + LA + j * 8192 + 4096 + hh * 256 + (l & 15) * 16;
    const unsigned long long vmask = 0x0000ffff0000ffffull;
    const unsigned kvs = WS_KV + (hp * 2 + j) * 131072 + t * 2048 - j * 8192, kvd = lds0 + LA + j * 8192 + t * 2048;
#define AARGS SA, SB, OA, Kf, V0, V1, Qf, sidx, kaddr, vaddr, vmask
    att_prime<0>(AARGS);
    att2<0, 3, 2>(AARGS);
    BARVN(0);
    if (w == 0) {
      const u32x4 m0 = *(LAS u32x4*)(size_t)(lds0 + T_KN + l * 32), m1 = *(LAS u32x4*)(size_t)(lds0 + T_KN + l * 32 + 16);
      unsigned m = umax2(umax2(umax2(m0[0], m0[1]), umax2(m0[2], m0[3])), umax2(umax2(m1[0], m1[1]), umax2(m1[2], m1[3])));
      m = dpp_max<0x121>(m); m = dpp_max<0x122>(m); m = dpp_max<0x124>(m); m = dpp_max<0x128>(m);
      const unsigned a0 = __builtin_amdgcn_readlane(m, 0), a1 = __builtin_amdgcn_readlane(m, 16), b0 = __builtin_amdgcn_readlane(m, 32), b1 = __builtin_amdgcn_readlane(m, 48);
      const unsigned lo = l ? b0 : a0, hi = l ? b1 : a1;
      const unsigned pr = (((lo & 0x7fffffffu) + 0xffffu) >> 16) | (((hi & 0x7fffffffu) + 0xffffu) & 0xffff0000u);
      if (l < 2) __hip_atomic_store((GAS unsigned long long*)(ws + WS_F1) + (hp * 16 + rt) * 2 + l, ((unsigned long long)tag << 32) | pr, RLX);
    }
    {
      const unsigned ta = lds0 + LW + w * 2048 + (4 * hh + q4) * 128 + (q4 >> 1) * 64 + blk * 32 + p4 * 8;
      const bf16x8 f0 = trf(ta, 1024), f1 = trf(ta ^ 64, 1024);
      const unsigned d0 = b < 32 ? WS_WO + (tpr * 32 + tks) * 2048 : b < 144 ? WS_W1 + (tpr * 64 + tks) * 1024 : WS_W2 + (tpr * 112 + tks) * 2048;
      __builtin_amdgcn_raw_buffer_store_b128(__builtin_bit_cast(u32x4, f0), sw, l * 16, d0, 16);
      __builtin_amdgcn_raw_buffer_store_b128(__builtin_bit_cast(u32x4, f1), sw, l * 16, d0 + ((b >= 32 && b < 144) ? 32768 : 1024), 16);
    }
    att2<2, 1, 0>(AARGS);
    unsigned kmx = 0;
    if (w == 0) {
      GAS unsigned long long* p = (GAS unsigned long long*)(ws + WS_F1) + hp * 32 + (l & 15) * 2 + blk;
      unsigned long long f = 0;
#pragma unroll 1
      for (unsigned sp = 0; sp < SPIN_CAP; ++sp) {
        f = __hip_atomic_load(p, RLX);
        if (__all((unsigned)(f >> 32) == tag)) break;
      }
      kmx = hh ? (unsigned)f & 0xffff0000u : (unsigned)f << 16;
    }
    BARL();
    STAMP(3);
#pragma unroll
    for (int c = 1; c < 4; c++) {
      const unsigned s = kvs + ((rt + c) & 15) * 8192;
      dma_sc1(kvd + c * 16384, kaddr, sw, s);
      dma_sc1(kvd + c * 16384 + 1024, kaddr, sw, s + 1024);
    }
    if (w == 0) {
      kmx = dpp_max<0x121>(kmx); kmx = dpp_max<0x122>(kmx); kmx = dpp_max<0x124>(kmx); kmx = dpp_max<0x128>(kmx);
      *(LAS unsigned*)(size_t)(lds0 + T_KALL + blk * 8 + hh * 4) = kmx;
    }
    BARV(4);
    if (w == 7 && l == 0) __hip_atomic_store((GAS unsigned*)(ws + WS_F5) + b, tag, RLX);
    bad = __any(!(qn * lds1(lds0 + T_KALL + j * 8 + hh * 4) <= 980.f));
    att_prime<4>(AARGS);
    STAMP(4);
    {
      unsigned w1t = (w7 ? w2t0 + 12288 : WS_W1 + (cs * 7 + w) * 32768) - j * 8192, w2t = w2t0 - j * 8192, rot = ((rt + 3) & 15) * 8192, t0, t1, ct;
      const unsigned wot = WS_WO + cs * 65536 + w * 8192 - j * 8192, f5 = WS_F5 - j * 8192, stg = lds0 + LB + w * 8192, ca = lds0 + T_CNT + j * 4;
      unsigned long long p0;
      asm volatile(ATT_MAIN_ASM
                   : ATT_OUT, "={v[64:79]}"(W1r[0]), "={v[80:95]}"(W1r[1]), "={v[96:111]}"(W1r[2]), "={v[112:127]}"(W1r[3]), "={v[128:143]}"(W1r[4]),
                     "={v[144:159]}"(W1r[5]), "={v[160:175]}"(W1r[6]), "={v[176:191]}"(W1r[7]), "={v[192:207]}"(W2r[0]), "={v[208:223]}"(W2r[1]),
                     "={v[224:239]}"(W2r[2]), [w1t] "+s"(w1t), [w2t] "+s"(w2t), [rot] "+s"(rot), [t0] "=&s"(t0), [t1] "=&s"(t1), [p0] "=&s"(p0), [ct] "={v240}"(ct)
                   : "{v[48:51]}"(Qf), [ix] "{v241}"(sidx), [ka] "{v242}"(kaddr), [va] "{v243}"(vaddr), [vm] "s"(vmask), [ca] "{v244}"(ca), [sw] "s"(sw),
                     [stg] "s"(stg), [wot] "s"(wot), [f5] "s"(f5), [tag] "s"(tag), [kvd] "s"(kvd), [kvs] "s"(kvs)
                   : "memory", "scc", "vcc");
    }
  }
  STAMP(5);
  int l;
  asm volatile("v_mbcnt_lo_u32_b32 %0, -1, 0\n\tv_mbcnt_hi_u32_b32 %0, -1, %0" : "=v"(l));
  const int r = l & 31, hh = l >> 5, blk = (l >> 4) & 1;
  const unsigned vl16 = l * 16;
  {
    float inv = __builtin_amdgcn_rcpf(OA[8]);
    float chk = OA[8] + inv;
#pragma unroll
    for (int i = 0; i < 8; i++) chk += fabsf(OA[i]);
    if (bad || __any(!(chk < 3.0e38f))) {
      float m = -3.0e38f;
#pragma unroll
      for (int i = 0; i < 16; i++) OA[i] = 0.f;
      const unsigned kb = WS_KV + (hp * 2 + j) * 131072;
#pragma unroll 1
      for (int n = 0; n < 64; n++) {
        const unsigned rb = kb + (n >> 2) * 8192 + (n & 3) * 1024;
        u32x4 kf = __builtin_amdgcn_raw_buffer_load_b128(sw, vl16, rb, 16);
        u32x4 va = __builtin_amdgcn_raw_buffer_load_b128(sw, 4096 + hh * 256 + (l & 15) * 16, rb, 16);
        u32x4 vb = __builtin_amdgcn_raw_buffer_load_b128(sw, 4096 + 512 + hh * 256 + (l & 15) * 16, rb, 16);
        if (blk) { va = (u32x4){0x3f803f80u, 0x3f803f80u, 0x3f803f80u, 0x3f803f80u}; vb = va; }
        f32x16 z;
#pragma unroll
        for (int i = 0; i < 16; i++) z[i] = 0.f;
        f32x16 s = MFMA(__builtin_bit_cast(bf16x8, kf), __builtin_bit_cast(bf16x8, Qf), z);
        float mx = s[0];
#pragma unroll
        for (int i = 1; i < 16; i++) mx = fmaxf(mx, s[i]);
        auto sw2 = __builtin_amdgcn_permlane32_swap(__float_as_uint(mx), __float_as_uint(mx), false, false);
        const float mn = fmaxf(m, fmaxf(__uint_as_float(sw2[0]), __uint_as_float(sw2[1]))), al = __builtin_amdgcn_exp2f(m - mn);
        m = mn;
        f32x8 p0, p1;
#pragma unroll
        for (int i = 0; i < 8; i++) { p0[i] = __builtin_amdgcn_exp2f(s[i] - mn); p1[i] = __builtin_amdgcn_exp2f(s[8 + i] - mn); }
#pragma unroll
        for (int i = 0; i < 16; i++) OA[i] *= al;
        OA = __builtin_amdgcn_smfmac_f32_32x32x32_bf16(__builtin_bit_cast(bf16x8, va), __builtin_bit_cast(bf16x16, p0), OA, 0xDDDD, 0, 0);
        OA = __builtin_amdgcn_smfmac_f32_32x32x32_bf16(__builtin_bit_cast(bf16x8, vb), __builtin_bit_cast(bf16x16, p1), OA, 0xDDDD, 0, 0);
      }
      inv = __builtin_amdgcn_rcpf(OA[8]);
    }
    u32x4 Op = {pk(OA[0] * inv, OA[1] * inv), pk(OA[2] * inv, OA[3] * inv), pk(OA[4] * inv, OA[5] * inv), pk(OA[6] * inv, OA[7] * inv)};
    __builtin_amdgcn_raw_buffer_store_b128(Op, sw, vl16, WS_O + O_OFF(rt * 4 + t, hp * 2 + j), 16);
  }
  BARV(0);
  if (w == 7 && l == 0) __hip_atomic_store((GAS unsigned*)(ws + WS_F2) + rt * 32 + hp, tag, RLX);
  STAMP(6);

  bf16x8 W1f[32];
#define UNP(n) W1f[4 * n] = sub4<0>(W1r[n]); W1f[4 * n + 1] = sub4<1>(W1r[n]); W1f[4 * n + 2] = sub4<2>(W1r[n]); W1f[4 * n + 3] = sub4<3>(W1r[n]);
  UNP(0) UNP(1) UNP(2) UNP(3) UNP(4) UNP(5) UNP(6) UNP(7)
  const int rt2 = w & 1, ct = (w >> 1) & 1;
  u32x2 xr[4];
#pragma unroll
  for (int g = 0; g < 4; g++) xr[g] = __builtin_amdgcn_raw_buffer_load_b64(sx, r * 1024 + hh * 8, (rg * 64 + rt2 * 32) * 1024 + cs * 128 + ct * 64 + g * 16, 0);
  float c1 = 0.f;
  if (!w7) {
#pragma unroll
    for (int i = 0; i < 32; i++) c1 = dot_ones(W1f[i], c1);
  } else wait_tags(ws + WS_F2 + (rg >> 1) * 128, 15, l, tag);
  f32x16 hacc;
#pragma unroll
  for (int g = 0; g < 4; g++) { hacc[4 * g] = blo(xr[g][0]); hacc[4 * g + 1] = bhi(xr[g][0]); hacc[4 * g + 2] = blo(xr[g][1]); hacc[4 * g + 3] = bhi(xr[g][1]); }
  asm volatile("" : "+v"(hacc));
  const unsigned oa = lds0 + LA + rt2 * 1024 + vl16, wa = lds0 + LB + ct * 1024 + vl16;
  bf16x8 wf[8];
#pragma unroll
  for (int k = 0; k < 8; k++) wf[k] = ldsf(wa + k * 2048);
  BARL();
  STAMP(7);
  u32x4 hres[2];
  {
    if (w < 4) {
      bf16x8 of[4];
#pragma unroll
      for (int n = 0; n < 32; n++) {
        if (n == 0) {
          SBAR();
#pragma unroll
          for (int k = 0; k < 4; k++) of[k] = ldsf(oa + k * 2048);
          SB0();
        }
        hacc = MFMA(wf[n & 7], of[n & 3], hacc);
        if (n + 4 == 8 || n + 4 == 16 || n + 4 == 24) SBAR();
        if (n + 4 < 32) of[n & 3] = ldsf(oa + (n + 4) * 2048);
        if (n + 8 < 32) wf[n & 7] = ldsf(wa + (n + 8) * 2048);
        SB0();
      }
      STAMP(14);
      float hv[16], s = 0.f, s2q = 0.f;
#pragma unroll
      for (int i = 0; i < 16; i++) hv[i] = hacc[i];
#pragma unroll
      for (int k = 0; k < 2; k++) {
        hres[k] = pk8(hv + 8 * k);
        __builtin_amdgcn_raw_buffer_store_b128(hres[k], sw, vl16, WS_H + H_OFF(rg * 2 + rt2, cs * 4 + ct * 2 + k), 16);
      }
      SB0();
#pragma unroll
      for (int i = 0; i < 16; i++) { s += hv[i]; s2q = fmaf(hv[i], hv[i], s2q); }
      s = xhalf_sum(s); s2q = xhalf_sum(s2q);
      __builtin_amdgcn_raw_buffer_store_b32(__float_as_uint(hh ? s2q : s), sw, r * 8 + hh * 4, WS_S + rg * 8192 + (cs * 2 + ct) * 512 + rt2 * 256, 16);
    } else {
#pragma unroll
      for (int i = 0; i < 16; i++) {
        dma_sc1(lds0 + LA + (i * 4 + t) * 1024, vl16, sw, WS_O + rg * 65536 + (i * 4 + t) * 1024);
        if (i == 11) BARVN(8);
      }
      BARVN(8); BARVN(4); BARVN(0);
      hres[0] = (u32x4){0, 0, 0, 0}; hres[1] = hres[0];
    }
  }
  BARV(0);
  if (w == 7 && l == 0) __hip_atomic_store((GAS unsigned*)(ws + WS_F3) + rg * 32 + cs, tag, RLX);
  STAMP(8);

  if (w7) wait_tags(ws + WS_F3 + rg * 128, 7, l, tag);
  asm volatile("s_barrier" ::: "memory");
  STAMP(9);
#pragma unroll
  for (int i = 0; i < 2; i++) dma_sc1(lds0 + LA + (i * 8 + w) * 1024, vl16, sw, WS_H + rg * 65536 + (i * 8 + w) * 1024);
  if (w7) {
    float s = 0.f, s2q = 0.f;
    u32x2 pr[16];
#pragma unroll
    for (int i = 16; i < 64; i++) {
      dma_sc1(lds0 + LA + i * 1024, vl16, sw, WS_H + rg * 65536 + i * 1024);
      if (i == 31) BARVN(16);
      if (i == 47) BARVN(16);
    }
    BARVN(16);
#pragma unroll
    for (int i = 0; i < 16; i++) pr[i] = __builtin_amdgcn_raw_buffer_load_b64(sw, l * 8, WS_S + rg * 8192 + i * 512, 16);
    BARVN(16);
#pragma unroll
    for (int i = 0; i < 16; i++) { s += __uint_as_float(pr[i][0]); s2q += __uint_as_float(pr[i][1]); }
    const float mean = s * (1.f / 512.f), var = fmaxf(s2q * (1.f / 512.f) - mean * mean, 0.f);
    const float ra = __builtin_amdgcn_rsqf(var + 1e-5f);
    *(LAS f32x2*)(size_t)(lds0 + T_ST2 + l * 8) = (f32x2){ra, -mean};
    BARL();
  } else {
    f32x16 a0, a1;
#pragma unroll
    for (int i = 0; i < 16; i++) { a0[i] = 0.f; a1[i] = 0.f; }
    const unsigned ha = lds0 + LA + vl16;
    bf16x8 hf[6];
#pragma unroll
    for (int n = 0; n < 64; n++) {
      if (n == 0) {
        BARVN(0);
#pragma unroll
        for (int m = 0; m < 6; m++) hf[m] = ldsf(ha + m * 1024);
        SB0();
      }
      if (n & 1) a1 = MFMA(W1f[n >> 1], hf[n % 6], a1); else a0 = MFMA(W1f[n >> 1], hf[n % 6], a0);
      if (n + 6 == 16 || n + 6 == 32 || n + 6 == 48) SBAR();
      if (n + 6 < 64) hf[n % 6] = ldsf(ha + (n + 6) * 1024);
      if (n >= 15 && n < 61 && n % 3 == 0) W1f[n / 3 - 5] = __builtin_bit_cast(bf16x8, __builtin_amdgcn_raw_buffer_load_b128(sw, vl16, w2t0 + 12288 + (n / 3 - 5) * 1024, 16));
      SB0();
    }
    STAMP(15);
    BARL();
    const f32x2 st0 = *(LAS f32x2*)(size_t)(lds0 + T_ST2 + r * 8), st1 = *(LAS f32x2*)(size_t)(lds0 + T_ST2 + 256 + r * 8);
    a0 = MFMA32(c1, st0[1], a0);
    a1 = MFMA32(c1, st1[1], a1);
    u32x4 up[4];
#pragma unroll
    for (int i = 0; i < 8; i++) {
      up[i >> 2][i & 3] = relu2(pk(st0[0] * a0[2 * i], st0[0] * a0[2 * i + 1]));
      up[2 + (i >> 2)][i & 3] = relu2(pk(st1[0] * a1[2 * i], st1[0] * a1[2 * i + 1]));
    }
    const unsigned ub = WS_U + (rg * 8 + cs) * 28672 + w * 2048;
#pragma unroll
    for (int k = 0; k < 2; k++) {
      __builtin_amdgcn_raw_buffer_store_b128(up[k], sw, vl16, ub + k * 1024, 16);
      __builtin_amdgcn_raw_buffer_store_b128(up[2 + k], sw, vl16, ub + 14336 + k * 1024, 16);
    }
  }
  BARV(0);
  if (w == 7 && l == 0) __hip_atomic_store((GAS unsigned*)(ws + WS_F4) + rg * 32 + cs, tag, RLX);
  STAMP(10);

  if (w7) wait_tags(ws + WS_F4 + rg * 128, 7, l, tag);
  f32x16 acc[4];
#pragma unroll
  for (int q = 0; q < 4; q++) {
    const float f = (w == q) ? 1.f : 0.f;
#pragma unroll
    for (int i = 0; i < 8; i++) { acc[q][2 * i] = f * blo(hres[i >> 2][i & 3]); acc[q][2 * i + 1] = f * bhi(hres[i >> 2][i & 3]); }
  }
  asm volatile("" : "+v"(acc[0]), "+v"(acc[1]), "+v"(acc[2]), "+v"(acc[3]));
  BARL();
  STAMP(11);
  {
    bf16x8 W2f[28];
#define UNP2(n) W2f[4 * n] = sub4<0>(W2r[n]); W2f[4 * n + 1] = sub4<1>(W2r[n]); W2f[4 * n + 2] = sub4<2>(W2r[n]); W2f[4 * n + 3] = sub4<3>(W2r[n]);
    UNP2(0) UNP2(1) UNP2(2)
#pragma unroll
    for (int i = 0; i < 16; i++) W2f[12 + i] = W1f[i];
    const unsigned ub = WS_U + (rg * 8 + w) * 28672;
    const unsigned pw = lds0 + w * 16384 + r * 256;
    constexpr int NU = FNU;
    u32x4 uf[NU];
#pragma unroll
    for (int n = 0; n < NU; n++) uf[n] = __builtin_amdgcn_raw_buffer_load_b128(sw, vl16, ub + n * 1024, 16);
    SB0();
#pragma unroll
    for (int n = 0; n < 28; n++) {
      const int rr = n / 14, k = n % 14;
      acc[rr] = MFMA(W2f[2 * k], __builtin_bit_cast(bf16x8, uf[n % NU]), acc[rr]);
      acc[rr + 2] = MFMA(W2f[2 * k + 1], __builtin_bit_cast(bf16x8, uf[n % NU]), acc[rr + 2]);
      if (n + NU < 28) uf[n % NU] = __builtin_amdgcn_raw_buffer_load_b128(sw, vl16, ub + (n + NU) * 1024, 16);
      if (n >= 16 && n < 24) {
        const int c2 = (n - 16) >> 2, g = (n - 16) & 3;
        const f32x16& a = acc[2 * c2];
        *(LAS f32x4*)(size_t)(pw + (((c2 * 8 + g * 2 + hh) ^ (r & 7)) << 4)) = (f32x4){a[4 * g], a[4 * g + 1], a[4 * g + 2], a[4 * g + 3]};
      }
      SB0();
    }
#pragma unroll
    for (int c2 = 0; c2 < 2; c2++)
#pragma unroll
      for (int g = 0; g < 4; g++) {
        const f32x16& a = acc[1 + 2 * c2];
        *(LAS f32x4*)(size_t)(pw + 8192 + (((c2 * 8 + g * 2 + hh) ^ (r & 7)) << 4)) = (f32x4){a[4 * g], a[4 * g + 1], a[4 * g + 2], a[4 * g + 3]};
      }
  }
  BARL();
  STAMP(12);
  {
    const int row = w * 8 + (l >> 3), cg = l & 7;
    const unsigned a0 = lds0 + row * 256 + (((2 * cg) ^ (row & 7)) << 4), a1 = a0 ^ 16;
    f32x4 s0 = lds4(a0), s1 = lds4(a1);
#pragma unroll
    for (int p = 1; p < 8; p++) { s0 += lds4(a0 + p * 16384); s1 += lds4(a1 + p * 16384); }
    u32x4 o = {pk(s0[0], s0[1]), pk(s0[2], s0[3]), pk(s1[0], s1[1]), pk(s1[2], s1[3])};
    *(u32x4*)(out + (rg * 64 + row) * 512 + cs * 64 + cg * 8) = o;
  }
  if (b == 0 && w == 0 && l == 0) __hip_atomic_store((GAS unsigned*)ws, tag, RLX);
}

extern "C" void kernel_launch(void* const* d_in, const int* in_sizes, int n_in, void* d_out, int out_size, void* d_ws, size_t ws_size, hipStream_t stream) {
  fused_block<<<256, 512, 0, stream>>>((const u16*)d_in[0], (const u16*)d_in[1], (const u16*)d_in[2], (const u16*)d_in[4], (const u16*)d_in[5], (u16*)d_out, (char*)d_ws);
}
```
